```python
import torch

def _softmax(x, axis=-1):
    return torch.softmax(x, dim=axis)
def _gelu(x):
    return torch.nn.functional.gelu(x, approximate="tanh")
def _silu(x):
    return torch.nn.functional.silu(x)
def _sigmoid(x):
    return torch.sigmoid(x)
def _layernorm(x, axis=-1, eps=1e-5):
    m = x.mean(dim=axis, keepdim=True)
    v = x.var(dim=axis, keepdim=True, unbiased=False)
    return (x - m) / torch.sqrt(v + eps)
def _scatter_add(tgt, idx, src, axis=0):
    return tgt.clone().index_add_(axis, idx, src)
def _segment_sum(src, seg, n):
    out = torch.zeros((n,) + src.shape[1:], dtype=src.dtype)
    return out.index_add_(0, seg, src)
def _pad(x, pw):
    flat = tuple(v for pair in reversed(pw) for v in pair)
    return torch.nn.functional.pad(x, flat)

kernel_name = 'interleave_layers__ssm_conv_h3__dense_activation_silu__prompt'

def setup_inputs():

    _w = (lambda a: (lambda t: t.to(torch.bfloat16) if t.is_floating_point() else t)(torch.as_tensor(a)))
    def _A_setup():
        B, S, D = 1, 2048, 640
        ks = 4
        return {
            "q": _w((lambda *s: torch.randn(*s, dtype=torch.bfloat16) * 0.05)(B, S, D)),
            "k": _w((lambda *s: torch.randn(*s, dtype=torch.bfloat16) * 0.2)(B, S, D)),
            "v": _w((lambda *s: torch.randn(*s, dtype=torch.bfloat16) * 0.2)(B, S, D)),
            "shift_k": _w((lambda *s: torch.randn(*s, dtype=torch.bfloat16) * 0.2)(D, ks) * 0.5),
            "ssm_k": _w((lambda *s: torch.randn(*s, dtype=torch.bfloat16) * 0.8)(D, S) * (1.0 / S ** 0.5)),
            "ssm_D": _w((lambda *s: torch.randn(*s, dtype=torch.bfloat16) * 0.2)(D)),
        }
    _A_in = _A_setup()

    def _B_setup():
        B, S, D, F = 1, 2048, 640, 1728
        return {
            "x": _w((lambda *s: torch.randn(*s, dtype=torch.bfloat16) * 0.05)(B, S, D)),
            "w1": _w((lambda *s: torch.randn(*s, dtype=torch.bfloat16) * 0.8)(D, F) * (D ** -0.5)),
            "w2": _w((lambda *s: torch.randn(*s, dtype=torch.bfloat16) * 0.2)(F, D) * (F ** -0.5)),
        }
    _B_in = _B_setup()

    return {
        "A_q": _A_in["q"],
        "A_k": _A_in["k"],
        "A_v": _A_in["v"],
        "A_shift_k": _A_in["shift_k"],
        "A_ssm_k": _A_in["ssm_k"],
        "A_ssm_D": _A_in["ssm_D"],
        "B_x": _B_in["x"],
        "B_w1": _B_in["w1"],
        "B_w2": _B_in["w2"],
    }

def reference(A_q, A_k, A_v, A_shift_k, A_ssm_k, A_ssm_D, B_x, B_w1, B_w2):
    def _A_ref(q, k, v, shift_k, ssm_k, ssm_D):
        B, S, D = q.shape
        ks = shift_k.shape[-1]
        k_pad = _pad(k, ((0, 0), (ks - 1, 0), (0, 0)))
        k_shift = torch.zeros_like(k)
        for t in range(ks):
            k_shift = k_shift + k_pad[:, t:t + S, :] * shift_k[:, ks - 1 - t][None, None, :]
        kv = k_shift * v
        L = 2 * S
        kv_pad = _pad(kv, ((0, 0), (0, S), (0, 0)))
        kf_pad = _pad(ssm_k, ((0, 0), (0, S)))
        kv_f = (lambda x, axes=(-2,-1): torch.fft.rfft2(x.float(), dim=axes))(kv_pad[:, None, :, :], axes=(1, 2))
        kf_f = (lambda x, axes=(-2,-1): torch.fft.rfft2(x.float(), dim=axes))(kf_pad[:, None, :], axes=(1, 2))
        kf_f = (lambda x, *d: x.permute(*d))(kf_f, 1, 2, 0)
        y_f = kv_f * kf_f[None, :, :, :]
        y = (lambda x, s, axes=(-2,-1): torch.fft.irfft2(x, s=s, dim=axes).to(torch.bfloat16))(y_f, (1, L), axes=(1, 2))[:, 0, :S, :]
        y = y + kv * ssm_D[None, None, :]
        return q * y

    def _B_ref(x, w1, w2):
        h = torch.einsum("bsd,df->bsf", x, w1)
        h = _silu(h)
        out = torch.einsum("bsf,fd->bsd", h, w2)
        return out

    x = A_q
    x = x + _A_ref(_layernorm(x, axis=-1), A_k, A_v, A_shift_k, A_ssm_k, A_ssm_D)
    x = x + _B_ref(_layernorm(x, axis=-1), B_w1, B_w2)
    x = x + _A_ref(_layernorm(x, axis=-1), A_k, A_v, A_shift_k, A_ssm_k, A_ssm_D)
    x = x + _B_ref(_layernorm(x, axis=-1), B_w1, B_w2)
    return x
```

```cpp
#include <hip/hip_runtime.h>
#include <hip/hip_bf16.h>
#include <stdint.h>

#define GAS __attribute__((address_space(1)))
#define LAS __attribute__((address_space(3)))
typedef unsigned v4u __attribute__((ext_vector_type(4)));
typedef unsigned v2u __attribute__((ext_vector_type(2)));
typedef float f32x4 __attribute__((ext_vector_type(4)));
typedef float f32x2 __attribute__((ext_vector_type(2)));
typedef short bf16x8 __attribute__((ext_vector_type(8)));
typedef short s16x4 __attribute__((ext_vector_type(4)));
typedef __bf16 bf16x2_t __attribute__((ext_vector_type(2)));
typedef unsigned short u16;

namespace {

constexpr int S = 2048, D = 640, F = 1728;
constexpr int KS1 = D / 32;
constexpr int KS2 = F / 32;
constexpr size_t WF_BYTES = (size_t)D * F * 2;
constexpr size_t XF_BYTES = (size_t)S * D * 2;
constexpr size_t HF_BYTES = (size_t)S * F * 2;
constexpr size_t W1F_OFF = 0;
constexpr size_t W2F_OFF = W1F_OFF + WF_BYTES;
constexpr size_t YF_OFF = W2F_OFF + WF_BYTES;
constexpr size_t X1F_OFF = YF_OFF + XF_BYTES;
constexpr size_t X2F_OFF = X1F_OFF + XF_BYTES;
constexpr size_t X3F_OFF = X2F_OFF + XF_BYTES;
constexpr size_t HF_OFF = X3F_OFF + XF_BYTES;

__device__ __forceinline__ unsigned pack2(float a, float b) {
  f32x2 v = {a, b};
  bf16x2_t r = __builtin_convertvector(v, bf16x2_t);
  return __builtin_bit_cast(unsigned, r);
}
__device__ __forceinline__ float lo16(unsigned u) { return __builtin_bit_cast(float, u << 16); }
__device__ __forceinline__ float hi16(unsigned u) { return __builtin_bit_cast(float, u & 0xffff0000u); }
__device__ __forceinline__ float rbf(float a) { return lo16(pack2(a, a)); }
__device__ __forceinline__ float bf2f(u16 b) { return __builtin_bit_cast(float, ((unsigned)b) << 16); }
__device__ __forceinline__ f32x4 mfma(v4u a, v4u b, f32x4 c) {
  return __builtin_amdgcn_mfma_f32_16x16x32_bf16(__builtin_bit_cast(bf16x8, a), __builtin_bit_cast(bf16x8, b), c, 0, 0, 0);
}
__device__ __forceinline__ v2u tr_read(LAS unsigned char* p) {
  return __builtin_bit_cast(v2u, __builtin_amdgcn_ds_read_tr16_b64_v4i16((LAS s16x4*)p));
}

constexpr int CONV_WGS = 160, PREP_WGS = 96;
constexpr int XR_BYTES = 4608;
constexpr int RS = 4160;
constexpr int CH_BYTES = XR_BYTES + 4 * RS;
constexpr int K1_LDS = 4 * CH_BYTES;

__global__ __launch_bounds__(256, 1) void k_conv_prep(const u16* __restrict__ gk, const u16* __restrict__ gv,
                                                      const u16* __restrict__ gshift, const u16* __restrict__ gssm,
                                                      const u16* __restrict__ gD, const u16* __restrict__ gw1,
                                                      const u16* __restrict__ gw2, unsigned char* __restrict__ ws) {
  extern __shared__ __attribute__((aligned(16))) unsigned char smem_raw[];
  LAS unsigned char* smem = (LAS unsigned char*)smem_raw;
  const int b = blockIdx.x, tid = threadIdx.x, lane = tid & 63;
  const int w = __builtin_amdgcn_readfirstlane(tid >> 6);
  if (b < CONV_WGS) {
    const int cg = (b & 7) * 20 + (b >> 3), d0 = 4 * cg;
    const u16* hrow = gssm + (size_t)(d0 + w) * S;
    v4u ha[4];
    v2u hn[4];
#pragma unroll
    for (int u = 0; u < 4; ++u) {
      const int c = 64 * u + lane;
      ha[u] = *(const v4u*)(hrow + 8 * c);
      hn[u] = v2u{0u, 0u};
      if (c < 255) hn[u] = *(const v2u*)(hrow + 8 * c + 8);
    }
    v2u kr[11], vr[8];
#pragma unroll
    for (int r = 0; r < 11; ++r) {
      const int t = 8 * tid - 3 + r;
      kr[r] = v2u{0u, 0u};
      if (t >= 0) kr[r] = *(const v2u*)(gk + (size_t)t * D + d0);
    }
#pragma unroll
    for (int r = 0; r < 8; ++r) vr[r] = *(const v2u*)(gv + (size_t)(8 * tid + r) * D + d0);
    __builtin_amdgcn_sched_barrier(0);

    LAS unsigned char* chb = smem + w * CH_BYTES;
    LAS unsigned char* hs = chb + XR_BYTES;
#pragma unroll
    for (int u = 0; u < 4; ++u) {
      const int c = 64 * u + lane;
      const v4u a = ha[u];
      const v2u n = hn[u];
      const unsigned s01 = __builtin_amdgcn_alignbyte(a.y, a.x, 2), s12 = __builtin_amdgcn_alignbyte(a.z, a.y, 2),
                     s23 = __builtin_amdgcn_alignbyte(a.w, a.z, 2), s34 = __builtin_amdgcn_alignbyte(n.x, a.w, 2),
                     s45 = __builtin_amdgcn_alignbyte(n.y, n.x, 2);
      LAS unsigned char* dst = hs + (c + 2) * 16;
      *(LAS v4u*)(dst) = a;
      *(LAS v4u*)(dst + RS) = v4u{s01, s12, s23, s34};
      *(LAS v4u*)(dst + 2 * RS) = v4u{a.y, a.z, a.w, n.x};
      *(LAS v4u*)(dst + 3 * RS) = v4u{s12, s23, s34, s45};
    }
    {
      const unsigned f0 = __builtin_amdgcn_readfirstlane(ha[0].x), f1 = __builtin_amdgcn_readfirstlane(ha[0].y);
      if (lane < 2) {
        const unsigned n0 = lane ? f0 : 0u, n1 = lane ? f1 : 0u;
        LAS unsigned char* dst = hs + lane * 16;
        *(LAS v4u*)(dst) = v4u{0u, 0u, 0u, 0u};
        *(LAS v4u*)(dst + RS) = v4u{0u, 0u, 0u, n0 << 16};
        *(LAS v4u*)(dst + 2 * RS) = v4u{0u, 0u, 0u, n0};
        *(LAS v4u*)(dst + 3 * RS) = v4u{0u, 0u, n0 << 16, __builtin_amdgcn_alignbyte(n1, n0, 2)};
      }
      if (lane < 30) *(LAS v4u*)(chb + 4096 + lane * 16) = v4u{0u, 0u, 0u, 0u};
    }
    {
      const v4u sw0 = *(const v4u*)(gshift + d0 * 4), sw1 = *(const v4u*)(gshift + d0 * 4 + 8);
      float wsh[4][4];
      wsh[0][0] = lo16(sw0.x); wsh[0][1] = hi16(sw0.x); wsh[0][2] = lo16(sw0.y); wsh[0][3] = hi16(sw0.y);
      wsh[1][0] = lo16(sw0.z); wsh[1][1] = hi16(sw0.z); wsh[1][2] = lo16(sw0.w); wsh[1][3] = hi16(sw0.w);
      wsh[2][0] = lo16(sw1.x); wsh[2][1] = hi16(sw1.x); wsh[2][2] = lo16(sw1.y); wsh[2][3] = hi16(sw1.y);
      wsh[3][0] = lo16(sw1.z); wsh[3][1] = hi16(sw1.z); wsh[3][2] = lo16(sw1.w); wsh[3][3] = hi16(sw1.w);
#pragma unroll
      for (int ch = 0; ch < 4; ++ch) {
        float kf[11], kvv[8];
#pragma unroll
        for (int r = 0; r < 11; ++r) {
          const unsigned u = (ch & 2) ? kr[r].y : kr[r].x;
          kf[r] = (ch & 1) ? hi16(u) : lo16(u);
        }
#pragma unroll
        for (int r = 0; r < 8; ++r) {
          const unsigned u = (ch & 2) ? vr[r].y : vr[r].x;
          const float vf = (ch & 1) ? hi16(u) : lo16(u);
          float ks = kf[r] * wsh[ch][3];
          ks = fmaf(kf[r + 1], wsh[ch][2], ks);
          ks = fmaf(kf[r + 2], wsh[ch][1], ks);
          ks = fmaf(kf[r + 3], wsh[ch][0], ks);
          kvv[r] = rbf(ks) * vf;
        }
        v4u o;
        o.x = pack2(kvv[7], kvv[6]);
        o.y = pack2(kvv[5], kvv[4]);
        o.z = pack2(kvv[3], kvv[2]);
        o.w = pack2(kvv[1], kvv[0]);
        *(LAS v4u*)(smem + ch * CH_BYTES + (2040 - 8 * tid) * 2) = o;
      }
    }
    __syncthreads();
    const int i16 = lane & 15, q = lane >> 4;
    const int sig = (1 + i16) & 3;
    LAS unsigned char* abase = hs + sig * RS + 2 * (1 + i16 - sig) + 16 * q;
    LAS unsigned char* bbase = chb + 480 - 32 * i16 + 16 * q;
    f32x4 acc[8];
#pragma unroll
    for (int n = 0; n < 8; ++n) acc[n] = f32x4{0.f, 0.f, 0.f, 0.f};
#pragma unroll
    for (int pass = 0; pass < 2; ++pass) {
      v4u A[32];
#pragma unroll
      for (int c = 0; c < 32; ++c) {
        const v2u lo = *(LAS v2u*)(abase + 64 * (32 * pass + c));
        const v2u hi = *(LAS v2u*)(abase + 64 * (32 * pass + c) + 8);
        A[c] = v4u{lo.x, lo.y, hi.x, hi.y};
      }
#pragma unroll
      for (int v = 0; v < 8 - 4 * pass; ++v) {
        v4u B[8];
#pragma unroll
        for (int ww = 0; ww < 8; ++ww) B[ww] = *(LAS v4u*)(bbase + 64 * (56 - (8 * v - ww)));
#pragma unroll
        for (int uu = 0; uu < 4; ++uu) {
          const int u = 4 * pass + uu;
          if (u + v < 8) {
#pragma unroll
            for (int ww = 0; ww < 8; ++ww) acc[u + v] = mfma(A[8 * uu + ww], B[ww], acc[u + v]);
          }
        }
      }
    }
    const float Dv = bf2f(gD[d0 + w]);
    LAS unsigned char* yt = hs + 64 * w;
#pragma unroll
    for (int n = 0; n < 8; ++n) {
      const int s0 = 256 * n + 16 * i16 + 4 * q;
      const v2u kk = *(LAS v2u*)(chb + (2044 - s0) * 2);
      const float y0 = rbf(acc[n][0]) + rbf(hi16(kk.y) * Dv);
      const float y1 = rbf(acc[n][1]) + rbf(lo16(kk.y) * Dv);
      const float y2 = rbf(acc[n][2]) + rbf(hi16(kk.x) * Dv);
      const float y3 = rbf(acc[n][3]) + rbf(lo16(kk.x) * Dv);
      *(LAS v2u*)(yt + s0 * 2) = v2u{pack2(y0, y1), pack2(y2, y3)};
    }
    __syncthreads();
    {
      const int qq = (lane & 15) >> 2, pp = lane & 3;
      LAS unsigned char* tb = smem + qq * CH_BYTES + XR_BYTES + 64 * qq + (16 * q + 4 * pp) * 2;
      unsigned char* yf = ws + YF_OFF + (size_t)(d0 >> 5) * 1024 + ((d0 >> 3) & 3) * 256 + ((d0 >> 2) & 1) * 8 + (lane & 15) * 16;
#pragma unroll
      for (int it = 0; it < 8; ++it) {
        const int tok0 = (w * 8 + it) * 64;
        const v2u r = tr_read(tb + tok0 * 2);
        const int tt = (tok0 >> 4) + q;
        *(v2u*)(yf + (size_t)tt * (KS1 * 1024)) = r;
      }
    }
  } else {
    const int gw = (b - CONV_WGS) * 4 + w;
    LAS unsigned char* tl = smem + w * (3 * 32 * 144);
    v4u ld[3][4];
    const int r8 = lane >> 3, c8 = lane & 7;
#pragma unroll
    for (int r = 0; r < 3; ++r) {
      const int id = gw + 384 * r;
      if (id < 1080) {
        const u16* src;
        int ldm;
        if (id < 540) {
          src = gw1 + (size_t)(id / 27) * 32 * F + (id % 27) * 64;
          ldm = F;
        } else {
          src = gw2 + (size_t)((id - 540) / 10) * 32 * D + ((id - 540) % 10) * 64;
          ldm = D;
        }
#pragma unroll
        for (int u = 0; u < 4; ++u) ld[r][u] = *(const v4u*)(src + (size_t)(8 * u + r8) * ldm + c8 * 8);
      }
    }
    const int g = lane >> 4, qq = (lane & 15) >> 2, pp = lane & 3;
#pragma unroll
    for (int r = 0; r < 3; ++r) {
      const int id = gw + 384 * r;
      if (id < 1080) {
        LAS unsigned char* tr = tl + r * (32 * 144);
#pragma unroll
        for (int u = 0; u < 4; ++u) *(LAS v4u*)(tr + (8 * u + r8) * 144 + c8 * 16) = ld[r][u];
        __builtin_amdgcn_fence(__ATOMIC_RELEASE, "wavefront");
        __builtin_amdgcn_wave_barrier();
        __builtin_amdgcn_fence(__ATOMIC_ACQUIRE, "wavefront");
        if (id < 540) {
          const int i = id / 27, nb = id % 27;
#pragma unroll
          for (int t = 0; t < 4; ++t) {
            const v2u lo = tr_read(tr + (8 * g + qq) * 144 + (16 * t + 4 * pp) * 2);
            const v2u hi = tr_read(tr + (8 * g + 4 + qq) * 144 + (16 * t + 4 * pp) * 2);
            const int T = 4 * nb + t;
            *(v4u*)(ws + W1F_OFF + (size_t)(((T >> 1) * KS1 + i) * 2 + (T & 1)) * 1024 + lane * 16) = v4u{lo.x, lo.y, hi.x, hi.y};
          }
        } else {
          const int i = (id - 540) / 10, db = (id - 540) % 10;
#pragma unroll
          for (int t = 0; t < 4; ++t) {
            const v2u lo = tr_read(tr + (4 * g + qq) * 144 + (16 * t + 4 * pp) * 2);
            const v2u hi = tr_read(tr + (16 + 4 * g + qq) * 144 + (16 * t + 4 * pp) * 2);
            const int T = 4 * db + t;
            *(v4u*)(ws + W2F_OFF + (size_t)(((T / 5) * KS2 + i) * 5 + (T % 5)) * 1024 + lane * 16) = v4u{lo.x, lo.y, hi.x, hi.y};
          }
        }
      }
    }
  }
}

constexpr int G1_PF = 14;
constexpr int G1_A_BYTES = 4 * KS1 * 1024;
constexpr int G1_LDS = G1_A_BYTES + 2 * 2 * 4 * 16 * 8;

__device__ __forceinline__ float red4(float x) {
  x += __shfl_xor(x, 16);
  x += __shfl_xor(x, 32);
  return x;
}

__device__ __forceinline__ f32x2 row_stats(const float (&x)[80], LAS f32x2* st_mine, LAS f32x2* st_other, bool writer) {
  float s = 0.f;
#pragma unroll
  for (int e = 0; e < 80; ++e) s += x[e];
  s = red4(s);
  const float ml = s * (1.f / 320.f);
  float m2 = 0.f;
#pragma unroll
  for (int e = 0; e < 80; ++e) {
    const float d = x[e] - ml;
    m2 = fmaf(d, d, m2);
  }
  m2 = red4(m2);
  if (writer) *st_mine = f32x2{ml, m2};
  __syncthreads();
  const f32x2 o = *st_other;
  const float mean = 0.5f * (ml + o.x);
  const float dl = ml - o.x;
  const float var = (m2 + o.y + dl * dl * 160.f) * (1.f / 640.f);
  const float mb = rbf(mean);
  const float sd = rbf(sqrtf(rbf(rbf(var) + 1e-5f)));
  return f32x2{mb, 1.f / sd};
}

template <bool FIRST>
__global__ __launch_bounds__(512, 2) void k_g1(const u16* __restrict__ gq, unsigned char* __restrict__ ws) {
  extern __shared__ __attribute__((aligned(16))) unsigned char smem_raw[];
  LAS unsigned char* smem = (LAS unsigned char*)smem_raw;
  const int b = blockIdx.x, tid = threadIdx.x, lane = tid & 63;
  const int w = __builtin_amdgcn_readfirstlane(tid >> 6);
  const int panel = (b & 7) * 4 + (b >> 6), slice = (b >> 3) & 7;
  const int pstart = slice < 6 ? 7 * slice : 42 + 6 * (slice - 6);
  const int npairs = slice < 6 ? 7 : 6;
  const int tt = w & 3, kh = w >> 2;
  const int tok = lane & 15, g = lane >> 4;
  v4u xr[10], yr[10];
  {
    const v4u* yp = (const v4u*)(ws + YF_OFF) + ((size_t)(4 * panel + tt) * KS1 + 10 * kh) * 64 + lane;
    if (FIRST) {
      const u16* xp = gq + (size_t)(64 * panel + 16 * tt + tok) * D + 320 * kh + 8 * g;
#pragma unroll
      for (int i = 0; i < 10; ++i) xr[i] = *(const v4u*)(xp + 32 * i);
    } else {
      const v4u* xp = (const v4u*)(ws + X2F_OFF) + ((size_t)(4 * panel + tt) * KS1 + 10 * kh) * 64 + lane;
#pragma unroll
      for (int i = 0; i < 10; ++i) xr[i] = xp[i * 64];
    }
#pragma unroll
    for (int i = 0; i < 10; ++i) yr[i] = yp[i * 64];
  }
  __builtin_amdgcn_sched_barrier(0);
  const bool active = w < npairs;
  const int P = pstart + (active ? w : 0);
  const v4u* wp = (const v4u*)(ws + W1F_OFF) + (size_t)P * (KS1 * 128) + lane;
  v4u wf[KS1][2];
#pragma unroll
  for (int i = 0; i < G1_PF; ++i) {
    wf[i][0] = wp[(2 * i) * 64];
    wf[i][1] = wp[(2 * i + 1) * 64];
  }
  __builtin_amdgcn_sched_barrier(0);
  LAS f32x2* st = (LAS f32x2*)(smem + G1_A_BYTES);
  {
    float x[80];
#pragma unroll
    for (int i = 0; i < 10; ++i) {
#pragma unroll
      for (int c = 0; c < 4; ++c) {
        x[8 * i + 2 * c] = lo16(xr[i][c]);
        x[8 * i + 2 * c + 1] = hi16(xr[i][c]);
      }
    }
    const f32x2 s1 = row_stats(x, st + ((0 * 2 + kh) * 4 + tt) * 16 + tok, st + ((0 * 2 + (kh ^ 1)) * 4 + tt) * 16 + tok, g == 0);
#pragma unroll
    for (int i = 0; i < 10; ++i) {
#pragma unroll
      for (int c = 0; c < 4; ++c) {
        const float a0 = fmaf((x[8 * i + 2 * c] - s1.x) * s1.y, lo16(yr[i][c]), x[8 * i + 2 * c]);
        const float a1 = fmaf((x[8 * i + 2 * c + 1] - s1.x) * s1.y, hi16(yr[i][c]), x[8 * i + 2 * c + 1]);
        const unsigned pk = pack2(a0, a1);
        xr[i][c] = pk;
        x[8 * i + 2 * c] = lo16(pk);
        x[8 * i + 2 * c + 1] = hi16(pk);
      }
    }
    if (w == slice) {
      v4u* xo = (v4u*)(ws + (FIRST ? X1F_OFF : X3F_OFF)) + ((size_t)(4 * panel + tt) * KS1 + 10 * kh) * 64 + lane;
#pragma unroll
      for (int i = 0; i < 10; ++i) xo[i * 64] = xr[i];
    }
    const f32x2 s2 = row_stats(x, st + ((1 * 2 + kh) * 4 + tt) * 16 + tok, st + ((1 * 2 + (kh ^ 1)) * 4 + tt) * 16 + tok, g == 0);
    LAS v4u* ap = (LAS v4u*)smem + (tt * KS1 + 10 * kh) * 64 + lane;
#pragma unroll
    for (int i = 0; i < 10; ++i) {
      v4u o;
#pragma unroll
      for (int c = 0; c < 4; ++c) o[c] = pack2((x[8 * i + 2 * c] - s2.x) * s2.y, (x[8 * i + 2 * c + 1] - s2.x) * s2.y);
      ap[i * 64] = o;
    }
  }
  __syncthreads();
  if (!active) return;
  f32x4 acc[2][4];
#pragma unroll
  for (int s = 0; s < 2; ++s)
#pragma unroll
    for (int t = 0; t < 4; ++t) acc[s][t] = f32x4{0.f, 0.f, 0.f, 0.f};
  const LAS v4u* ap = (const LAS v4u*)smem + lane;
#pragma unroll
  for (int i = 0; i < KS1; ++i) {
    if (i + G1_PF < KS1) {
      wf[i + G1_PF][0] = wp[(2 * (i + G1_PF)) * 64];
      wf[i + G1_PF][1] = wp[(2 * (i + G1_PF) + 1) * 64];
    }
    v4u a[4];
#pragma unroll
    for (int t = 0; t < 4; ++t) a[t] = ap[(t * KS1 + i) * 64];
#pragma unroll
    for (int t = 0; t < 4; ++t) {
      acc[0][t] = mfma(wf[i][0], a[t], acc[0][t]);
      acc[1][t] = mfma(wf[i][1], a[t], acc[1][t]);
    }
  }
  v4u* hp = (v4u*)(ws + HF_OFF) + ((size_t)(panel * KS2 + P) * 4) * 64 + lane;
#pragma unroll
  for (int t = 0; t < 4; ++t) {
    float hv[8];
#pragma unroll
    for (int s = 0; s < 2; ++s)
#pragma unroll
      for (int r = 0; r < 4; ++r) {
        const float z = rbf(acc[s][t][r]);
        hv[4 * s + r] = z * __builtin_amdgcn_rcpf(1.f + __expf(-z));
      }
    hp[t * 64] = v4u{pack2(hv[0], hv[1]), pack2(hv[2], hv[3]), pack2(hv[4], hv[5]), pack2(hv[6], hv[7])};
  }
}

constexpr int G2_PF = 6;
constexpr int G2_NK = 14;
constexpr int G2_LDS = 4 * 3 * 5 * 1024;

template <bool LAST>
__global__ __launch_bounds__(256, 1) void k_g2(unsigned char* __restrict__ ws, u16* __restrict__ gout) {
  extern __shared__ __attribute__((aligned(16))) unsigned char smem_raw[];
  LAS unsigned char* smem = (LAS unsigned char*)smem_raw;
  const int b = blockIdx.x, tid = threadIdx.x, lane = tid & 63;
  const int w = __builtin_amdgcn_readfirstlane(tid >> 6);
  const int panel = (b & 7) * 4 + (b >> 6), slice = (b >> 3) & 7;
  const int tok = lane & 15, q = lane >> 4;
  const v4u* hp = (const v4u*)(ws + HF_OFF) + ((size_t)(panel * KS2 + w) * 4) * 64 + lane;
  const v4u* wp = (const v4u*)(ws + W2F_OFF) + ((size_t)(slice * KS2 + w) * 5) * 64 + lane;
  const bool has_last = w < 2;
  v4u hf[G2_NK][4], wf[G2_NK][5];
#pragma unroll
  for (int i = 0; i < G2_PF; ++i) {
#pragma unroll
    for (int t = 0; t < 4; ++t) hf[i][t] = hp[(i * 16 + t) * 64];
#pragma unroll
    for (int t = 0; t < 5; ++t) wf[i][t] = wp[(i * 20 + t) * 64];
  }
  __builtin_amdgcn_sched_barrier(0);
  v2u xres[5];
  size_t xoff[5];
  {
    const unsigned char* xb = ws + (LAST ? X3F_OFF : X1F_OFF);
#pragma unroll
    for (int t = 0; t < 5; ++t) {
      const int T = 5 * slice + t;
      xoff[t] = ((size_t)(4 * panel + w) * KS1 + (T >> 1)) * 1024 + (tok + 16 * (2 * (T & 1) + (q >> 1))) * 16 + 8 * (q & 1);
      xres[t] = *(const v2u*)(xb + xoff[t]);
    }
  }
  __builtin_amdgcn_sched_barrier(0);
  f32x4 acc[4][5];
#pragma unroll
  for (int a = 0; a < 4; ++a)
#pragma unroll
    for (int t = 0; t < 5; ++t) acc[a][t] = f32x4{0.f, 0.f, 0.f, 0.f};
#pragma unroll
  for (int i = 0; i < G2_NK; ++i) {
    const int j = i + G2_PF;
    if (j < G2_NK) {
      if (j < G2_NK - 1 || has_last) {
#pragma unroll
        for (int t = 0; t < 4; ++t) hf[j][t] = hp[(j * 16 + t) * 64];
#pragma unroll
        for (int t = 0; t < 5; ++t) wf[j][t] = wp[(j * 20 + t) * 64];
      }
    }
    if (i < G2_NK - 1 || has_last) {
#pragma unroll
      for (int t = 0; t < 5; ++t)
#pragma unroll
        for (int a = 0; a < 4; ++a) acc[a][t] = mfma(wf[i][t], hf[i][a], acc[a][t]);
    }
    __builtin_amdgcn_sched_barrier(0);
  }
  LAS f32x4* red = (LAS f32x4*)smem;
#pragma unroll
  for (int a = 0; a < 4; ++a) {
    if (a != w) {
      const int slot = (w - a - 1) & 3;
#pragma unroll
      for (int t = 0; t < 5; ++t) red[((a * 3 + slot) * 5 + t) * 64 + lane] = acc[a][t];
    }
  }
  __syncthreads();
  f32x4 fin[5];
#pragma unroll
  for (int t = 0; t < 5; ++t) {
    fin[t] = w == 0 ? acc[0][t] : (w == 1 ? acc[1][t] : (w == 2 ? acc[2][t] : acc[3][t]));
#pragma unroll
    for (int s = 0; s < 3; ++s) fin[t] += red[((w * 3 + s) * 5 + t) * 64 + lane];
  }
#pragma unroll
  for (int t = 0; t < 5; ++t) {
    const float o0 = lo16(xres[t].x) + rbf(fin[t][0]);
    const float o1 = hi16(xres[t].x) + rbf(fin[t][1]);
    const float o2 = lo16(xres[t].y) + rbf(fin[t][2]);
    const float o3 = hi16(xres[t].y) + rbf(fin[t][3]);
    const v2u o = v2u{pack2(o0, o1), pack2(o2, o3)};
    if (LAST) {
      *(v2u*)(gout + (size_t)(64 * panel + 16 * w + tok) * D + 80 * slice + 16 * t + 4 * q) = o;
    } else {
      *(v2u*)(ws + X2F_OFF + xoff[t]) = o;
    }
  }
}

}

extern "C" void kernel_launch(void* const* d_in, const int* in_sizes, int n_in, void* d_out, int out_size, void* d_ws,
                              size_t ws_size, hipStream_t stream) {
  const u16* q = (const u16*)d_in[0];
  const u16* k = (const u16*)d_in[1];
  const u16* v = (const u16*)d_in[2];
  const u16* shift_k = (const u16*)d_in[3];
  const u16* ssm_k = (const u16*)d_in[4];
  const u16* ssm_D = (const u16*)d_in[5];
  const u16* w1 = (const u16*)d_in[7];
  const u16* w2 = (const u16*)d_in[8];
  unsigned char* ws = (unsigned char*)d_ws;
  u16* out = (u16*)d_out;
  (void)hipFuncSetAttribute((const void*)k_conv_prep, hipFuncAttributeMaxDynamicSharedMemorySize, K1_LDS);
  (void)hipFuncSetAttribute((const void*)k_g1<true>, hipFuncAttributeMaxDynamicSharedMemorySize, G1_LDS);
  (void)hipFuncSetAttribute((const void*)k_g1<false>, hipFuncAttributeMaxDynamicSharedMemorySize, G1_LDS);
  (void)hipFuncSetAttribute((const void*)k_g2<true>, hipFuncAttributeMaxDynamicSharedMemorySize, G2_LDS);
  (void)hipFuncSetAttribute((const void*)k_g2<false>, hipFuncAttributeMaxDynamicSharedMemorySize, G2_LDS);
  k_conv_prep<<<CONV_WGS + PREP_WGS, 256, K1_LDS, stream>>>(k, v, shift_k, ssm_k, ssm_D, w1, w2, ws);
  k_g1<true><<<256, 512, G1_LDS, stream>>>(q, ws);
  k_g2<false><<<256, 256, G2_LDS, stream>>>(ws, out);
  k_g1<false><<<256, 512, G1_LDS, stream>>>(q, ws);
  k_g2<true><<<256, 256, G2_LDS, stream>>>(ws, out);
}
```

```cpp
#include <hip/hip_runtime.h>
#include <hip/hip_bf16.h>
#include <stdint.h>

#define GAS __attribute__((address_space(1)))
#define LAS __attribute__((address_space(3)))
typedef unsigned v4u __attribute__((ext_vector_type(4)));
typedef unsigned v2u __attribute__((ext_vector_type(2)));
typedef float f32x4 __attribute__((ext_vector_type(4)));
typedef float f32x2 __attribute__((ext_vector_type(2)));
typedef short bf16x8 __attribute__((ext_vector_type(8)));
typedef short s16x4 __attribute__((ext_vector_type(4)));
typedef __bf16 bf16x2_t __attribute__((ext_vector_type(2)));
typedef unsigned short u16;

#define STAMP(n)
#define STAMP_DECL
#define STAMP_OUT(kid)

namespace {

constexpr int S = 2048, D = 640, F = 1728;
constexpr int KS1 = D / 32;
constexpr int KS2 = F / 32;
constexpr size_t WF_BYTES = (size_t)D * F * 2;
constexpr size_t XF_BYTES = (size_t)S * D * 2;
constexpr size_t HF_BYTES = (size_t)S * F * 2;
constexpr size_t W1F_OFF = 0;
constexpr size_t W2F_OFF = W1F_OFF + WF_BYTES;
constexpr size_t YF_OFF = W2F_OFF + WF_BYTES;
constexpr size_t X1F_OFF = YF_OFF + XF_BYTES;
constexpr size_t X2F_OFF = X1F_OFF + XF_BYTES;
constexpr size_t X3F_OFF = X2F_OFF + XF_BYTES;
constexpr size_t HF_OFF = X3F_OFF + XF_BYTES;
constexpr size_t KVT_OFF = HF_OFF + HF_BYTES;
constexpr size_t CS_OFF = KVT_OFF + XF_BYTES;

__device__ __forceinline__ unsigned pack2(float a, float b) {
  f32x2 v = {a, b};
  bf16x2_t r = __builtin_convertvector(v, bf16x2_t);
  return __builtin_bit_cast(unsigned, r);
}
__device__ __forceinline__ float lo16(unsigned u) { return __builtin_bit_cast(float, u << 16); }
__device__ __forceinline__ float hi16(unsigned u) { return __builtin_bit_cast(float, u & 0xffff0000u); }
__device__ __forceinline__ float rbf(float a) { return lo16(pack2(a, a)); }
__device__ __forceinline__ float bf2f(u16 b) { return __builtin_bit_cast(float, ((unsigned)b) << 16); }
__device__ __forceinline__ f32x4 mfma(v4u a, v4u b, f32x4 c) {
  return __builtin_amdgcn_mfma_f32_16x16x32_bf16(__builtin_bit_cast(bf16x8, a), __builtin_bit_cast(bf16x8, b), c, 0, 0, 0);
}
__device__ __forceinline__ v2u tr_read(LAS unsigned char* p) {
  return __builtin_bit_cast(v2u, __builtin_amdgcn_ds_read_tr16_b64_v4i16((LAS s16x4*)p));
}

constexpr int K0_RSTR = 144;
__global__ __launch_bounds__(256) void k_kv(const u16* __restrict__ gk, const u16* __restrict__ gv,
                                            const u16* __restrict__ gshift, unsigned char* __restrict__ ws) {
  __shared__ __attribute__((aligned(16))) unsigned char tile[64 * K0_RSTR];
  const int b = blockIdx.x, tid = threadIdx.x;
  const int t0 = (b & 31) * 64, d0 = (b >> 5) * 64;
  const int tr = tid >> 3, c8 = tid & 7;
  const int tb = t0 + 2 * tr, dc = d0 + 8 * c8;
  v4u kr[5], vr[2], sw[4];
#pragma unroll
  for (int r = 0; r < 5; ++r) {
    const int t = tb - 3 + r;
    kr[r] = *(const v4u*)(gk + (size_t)(t < 0 ? 0 : t) * D + dc);
  }
#pragma unroll
  for (int r = 0; r < 2; ++r) vr[r] = *(const v4u*)(gv + (size_t)(tb + r) * D + dc);
#pragma unroll
  for (int r = 0; r < 4; ++r) sw[r] = *(const v4u*)(gshift + dc * 4 + 8 * r);
#pragma unroll
  for (int r = 0; r < 3; ++r)
    if (tb - 3 + r < 0) kr[r] = v4u{0u, 0u, 0u, 0u};
#pragma unroll
  for (int j = 0; j < 8; ++j) {
    const unsigned wa = sw[j >> 1][2 * (j & 1)], wb = sw[j >> 1][2 * (j & 1) + 1];
    const float w0 = lo16(wa), w1 = hi16(wa), w2 = lo16(wb), w3 = hi16(wb);
    float kf[5];
#pragma unroll
    for (int r = 0; r < 5; ++r) kf[r] = (j & 1) ? hi16(kr[r][j >> 1]) : lo16(kr[r][j >> 1]);
    const float v0 = (j & 1) ? hi16(vr[0][j >> 1]) : lo16(vr[0][j >> 1]);
    const float v1 = (j & 1) ? hi16(vr[1][j >> 1]) : lo16(vr[1][j >> 1]);
    const float ks0 = fmaf(kf[3], w0, fmaf(kf[2], w1, fmaf(kf[1], w2, kf[0] * w3)));
    const float ks1 = fmaf(kf[4], w0, fmaf(kf[3], w1, fmaf(kf[2], w2, kf[1] * w3)));
    const unsigned kp = pack2(ks0, ks1);
    *(unsigned*)(tile + (8 * c8 + j) * K0_RSTR + 124 - 4 * tr) = pack2(hi16(kp) * v1, lo16(kp) * v0);
  }
  __syncthreads();
  const int ch = tid >> 2, part = tid & 3;
  const v4u o0 = *(const v4u*)(tile + ch * K0_RSTR + part * 32);
  const v4u o1 = *(const v4u*)(tile + ch * K0_RSTR + part * 32 + 16);
  v4u* dst = (v4u*)(ws + KVT_OFF + ((size_t)(d0 + ch) * S + (1984 - t0) + 16 * part) * 2);
  dst[0] = o0;
  dst[1] = o1;
}

constexpr int CONV_WGS = 160, PREP_WGS = 96;
constexpr int XR_BYTES = 4608;
constexpr int RS = 4160;
constexpr int CH_BYTES = XR_BYTES + 4 * RS;
constexpr int K1_LDS = 4 * 5 * 32 * 144;

#define WAVE_SYNC()                                      \
  do {                                                   \
    __builtin_amdgcn_fence(__ATOMIC_RELEASE, "wavefront"); \
    __builtin_amdgcn_wave_barrier();                     \
    __builtin_amdgcn_fence(__ATOMIC_ACQUIRE, "wavefront"); \
  } while (0)

__global__ __launch_bounds__(256, 1) void k_conv_prep(const u16* __restrict__ gssm, const u16* __restrict__ gD,
                                                      const u16* __restrict__ gw1, const u16* __restrict__ gw2,
                                                      unsigned char* __restrict__ ws) {
  extern __shared__ __attribute__((aligned(16))) unsigned char smem_raw[];
  LAS unsigned char* smem = (LAS unsigned char*)smem_raw;
  const int b = blockIdx.x, tid = threadIdx.x, lane = tid & 63;
  const int w = __builtin_amdgcn_readfirstlane(tid >> 6);
  STAMP_DECL
  STAMP(0);
  if (b < CONV_WGS) {
    const int cg = (b & 7) * 20 + (b >> 3), d0 = 4 * cg;
    const u16* hrow = gssm + (size_t)(d0 + w) * S;
    const v4u* xrow = (const v4u*)(ws + KVT_OFF + (size_t)(d0 + w) * S * 2);
    v4u ha[4], xa[4];
    v2u hn[4];
#pragma unroll
    for (int u = 0; u < 4; ++u) {
      const int c = 64 * u + lane;
      ha[u] = *(const v4u*)(hrow + 8 * c);
      hn[u] = *(const v2u*)(hrow + 8 * (c < 255 ? c + 1 : c));
    }
#pragma unroll
    for (int u = 0; u < 4; ++u) xa[u] = xrow[64 * u + lane];
    const unsigned Dbits = gD[d0 + w];
    __builtin_amdgcn_sched_barrier(0);
    STAMP(1);
    if (lane == 63) hn[3] = v2u{0u, 0u};

    LAS unsigned char* chb = smem + w * CH_BYTES;
    LAS unsigned char* hs = chb + XR_BYTES;
#pragma unroll
    for (int u = 0; u < 4; ++u) {
      const int c = 64 * u + lane;
      const v4u a = ha[u];
      const v2u n = hn[u];
      const unsigned s01 = __builtin_amdgcn_alignbyte(a.y, a.x, 2), s12 = __builtin_amdgcn_alignbyte(a.z, a.y, 2),
                     s23 = __builtin_amdgcn_alignbyte(a.w, a.z, 2), s34 = __builtin_amdgcn_alignbyte(n.x, a.w, 2),
                     s45 = __builtin_amdgcn_alignbyte(n.y, n.x, 2);
      LAS unsigned char* dst = hs + (c + 2) * 16;
      *(LAS v4u*)(dst) = a;
      *(LAS v4u*)(dst + RS) = v4u{s01, s12, s23, s34};
      *(LAS v4u*)(dst + 2 * RS) = v4u{a.y, a.z, a.w, n.x};
      *(LAS v4u*)(dst + 3 * RS) = v4u{s12, s23, s34, s45};
    }
    {
      const unsigned f0 = __builtin_amdgcn_readfirstlane(ha[0].x), f1 = __builtin_amdgcn_readfirstlane(ha[0].y);
      if (lane < 2) {
        const unsigned n0 = lane ? f0 : 0u, n1 = lane ? f1 : 0u;
        LAS unsigned char* dst = hs + lane * 16;
        *(LAS v4u*)(dst) = v4u{0u, 0u, 0u, 0u};
        *(LAS v4u*)(dst + RS) = v4u{0u, 0u, 0u, n0 << 16};
        *(LAS v4u*)(dst + 2 * RS) = v4u{0u, 0u, 0u, n0};
        *(LAS v4u*)(dst + 3 * RS) = v4u{0u, 0u, n0 << 16, __builtin_amdgcn_alignbyte(n1, n0, 2)};
      }
      if (lane < 30) *(LAS v4u*)(chb + 4096 + lane * 16) = v4u{0u, 0u, 0u, 0u};
    }
#pragma unroll
    for (int u = 0; u < 4; ++u) *(LAS v4u*)(chb + (64 * u + lane) * 16) = xa[u];
    WAVE_SYNC();
    STAMP(2);
    const int i16 = lane & 15, q = lane >> 4;
    const int sig = (1 + i16) & 3;
    LAS unsigned char* abase = hs + sig * RS + 2 * (1 + i16 - sig) + 16 * q;
    LAS unsigned char* bbase = chb + 480 - 32 * i16 + 16 * q;
    v4u B[2][8];
#pragma unroll
    for (int ww = 0; ww < 8; ++ww) B[0][ww] = *(LAS v4u*)(bbase + 64 * (56 + ww));
    v4u A[64];
#pragma unroll
    for (int c = 0; c < 64; ++c) {
      const v2u lo = *(LAS v2u*)(abase + 64 * c);
      const v2u hi = *(LAS v2u*)(abase + 64 * c + 8);
      A[c] = v4u{lo.x, lo.y, hi.x, hi.y};
    }
    v4u Ad;
    {
      const int j = 15 - i16 - 8 * q;
      const unsigned dv = (j & 1) ? (Dbits << 16) : Dbits;
#pragma unroll
      for (int c = 0; c < 4; ++c) Ad[c] = (j >= 0 && (j >> 1) == c) ? dv : 0u;
    }
    f32x4 acc[8];
#pragma unroll
    for (int n = 0; n < 8; ++n) acc[n] = f32x4{0.f, 0.f, 0.f, 0.f};
    STAMP(3);
#pragma unroll
    for (int v = 0; v < 8; ++v) {
      if (v < 7) {
#pragma unroll
        for (int ww = 0; ww < 8; ++ww) B[(v + 1) & 1][ww] = *(LAS v4u*)(bbase + 64 * (56 - (8 * (v + 1) - ww)));
      }
      __builtin_amdgcn_sched_barrier(0);
      acc[v] = mfma(Ad, B[v & 1][0], acc[v]);
#pragma unroll
      for (int u = 0; u < 8; ++u) {
        if (u + v < 8) {
#pragma unroll
          for (int ww = 0; ww < 8; ++ww) acc[u + v] = mfma(A[8 * u + ww], B[v & 1][ww], acc[u + v]);
        }
      }
      __builtin_amdgcn_sched_barrier(0);
    }
    STAMP(5);
    LAS unsigned char* yt = hs + 64 * w;
#pragma unroll
    for (int n = 0; n < 8; ++n) {
      const int s0 = 256 * n + 16 * i16 + 4 * q;
      *(LAS v2u*)(yt + s0 * 2) = v2u{pack2(acc[n][0], acc[n][1]), pack2(acc[n][2], acc[n][3])};
    }
    __syncthreads();
    {
      const int qq = (lane & 15) >> 2, pp = lane & 3;
      LAS unsigned char* tb = smem + qq * CH_BYTES + XR_BYTES + 64 * qq + (16 * q + 4 * pp) * 2;
      unsigned char* yf = ws + YF_OFF + (size_t)(d0 >> 5) * 1024 + ((d0 >> 3) & 3) * 256 + ((d0 >> 2) & 1) * 8 + (lane & 15) * 16;
#pragma unroll
      for (int it = 0; it < 8; ++it) {
        const int tok0 = (w * 8 + it) * 64;
        const v2u r = tr_read(tb + tok0 * 2);
        const int tt = (tok0 >> 4) + q;
        *(v2u*)(yf + (size_t)tt * (KS1 * 1024)) = r;
      }
    }
    STAMP(6);
    STAMP_OUT(0);
  } else {
    const int gw = (b - CONV_WGS) * 4 + w;
    LAS unsigned char* tl = smem + w * (5 * 32 * 144);
    const int r8 = lane >> 3, c8 = lane & 7;
    const int g = lane >> 4, qq = (lane & 15) >> 2, pp = lane & 3;
    if (gw < 108) {
      const int nb = gw % 27, kq = gw / 27;
      v4u ld[5][4];
#pragma unroll
      for (int r = 0; r < 5; ++r) {
        const u16* src = gw1 + (size_t)(5 * kq + r) * 32 * F + nb * 64;
#pragma unroll
        for (int u = 0; u < 4; ++u) ld[r][u] = *(const v4u*)(src + (size_t)(8 * u + r8) * F + c8 * 8);
      }
      f32x4 cs[4];
#pragma unroll
      for (int t = 0; t < 4; ++t) cs[t] = f32x4{0.f, 0.f, 0.f, 0.f};
      const v4u ones = v4u{0x3f803f80u, 0x3f803f80u, 0x3f803f80u, 0x3f803f80u};
#pragma unroll
      for (int r = 0; r < 5; ++r) {
        LAS unsigned char* tr = tl + r * (32 * 144);
#pragma unroll
        for (int u = 0; u < 4; ++u) *(LAS v4u*)(tr + (8 * u + r8) * 144 + c8 * 16) = ld[r][u];
        WAVE_SYNC();
        const int i = 5 * kq + r;
#pragma unroll
        for (int t = 0; t < 4; ++t) {
          const v2u lo = tr_read(tr + (8 * g + qq) * 144 + (16 * t + 4 * pp) * 2);
          const v2u hi = tr_read(tr + (8 * g + 4 + qq) * 144 + (16 * t + 4 * pp) * 2);
          const int T = 4 * nb + t;
          const v4u f = v4u{lo.x, lo.y, hi.x, hi.y};
          *(v4u*)(ws + W1F_OFF + (size_t)(((T >> 1) * KS1 + i) * 2 + (T & 1)) * 1024 + lane * 16) = f;
          cs[t] = mfma(f, ones, cs[t]);
        }
      }
      if ((lane & 15) == 0) {
#pragma unroll
        for (int t = 0; t < 4; ++t) *(f32x4*)(ws + CS_OFF + ((size_t)kq * F + nb * 64 + 16 * t + 4 * g) * 4) = cs[t];
      }
    } else if (gw < 378) {
      v4u ld[2][4];
#pragma unroll
      for (int r = 0; r < 2; ++r) {
        const int id = (gw - 108) * 2 + r;
        const u16* src = gw2 + (size_t)(id / 10) * 32 * D + (id % 10) * 64;
#pragma unroll
        for (int u = 0; u < 4; ++u) ld[r][u] = *(const v4u*)(src + (size_t)(8 * u + r8) * D + c8 * 8);
      }
#pragma unroll
      for (int r = 0; r < 2; ++r) {
        const int id = (gw - 108) * 2 + r;
        LAS unsigned char* tr = tl + r * (32 * 144);
#pragma unroll
        for (int u = 0; u < 4; ++u) *(LAS v4u*)(tr + (8 * u + r8) * 144 + c8 * 16) = ld[r][u];
        WAVE_SYNC();
        const int i = id / 10, db = id % 10;
#pragma unroll
        for (int t = 0; t < 4; ++t) {
          const v2u lo = tr_read(tr + (4 * g + qq) * 144 + (16 * t + 4 * pp) * 2);
          const v2u hi = tr_read(tr + (16 + 4 * g + qq) * 144 + (16 * t + 4 * pp) * 2);
          const int T = 4 * db + t;
          *(v4u*)(ws + W2F_OFF + (size_t)(((T / 5) * KS2 + i) * 5 + (T % 5)) * 1024 + lane * 16) = v4u{lo.x, lo.y, hi.x, hi.y};
        }
      }
    }
    STAMP(6);
    STAMP_OUT(0);
  }
}

constexpr int G1_PF = 14;
constexpr int G1_A_BYTES = 4 * KS1 * 1024;
constexpr int G1_LDS = G1_A_BYTES + 2 * 2 * 4 * 16 * 8;

__device__ __forceinline__ f32x2 finish_stats(f32x2 a, f32x2 b) {
  const float mean = (a.x + b.x) * (1.f / 640.f);
  const float var = fmaxf((a.y + b.y) * (1.f / 640.f) - mean * mean, 0.f);
  const float mb = rbf(mean);
  const float sd = rbf(__builtin_amdgcn_sqrtf(rbf(rbf(var) + 1e-5f)));
  return f32x2{mb, __builtin_amdgcn_rcpf(sd)};
}
__device__ __forceinline__ void part_stats(const v4u (&x)[10], LAS f32x2* dst, int tok, int g) {
  const v4u ones = v4u{0x3f803f80u, 0x3f803f80u, 0x3f803f80u, 0x3f803f80u};
  f32x4 sx = {0.f, 0.f, 0.f, 0.f}, gx = {0.f, 0.f, 0.f, 0.f};
#pragma unroll
  for (int i = 0; i < 10; ++i) {
    sx = mfma(ones, x[i], sx);
    gx = mfma(x[i], x[i], gx);
  }
  const int r = tok & 3;
  const float ss = r == 0 ? gx[0] : (r == 1 ? gx[1] : (r == 2 ? gx[2] : gx[3]));
  if (g == (tok >> 2)) *dst = f32x2{sx[0], ss};
}

template <bool FIRST>
__global__ __launch_bounds__(512, 2) void k_g1(const u16* __restrict__ gq, unsigned char* __restrict__ ws) {
  extern __shared__ __attribute__((aligned(16))) unsigned char smem_raw[];
  LAS unsigned char* smem = (LAS unsigned char*)smem_raw;
  const int b = blockIdx.x, tid = threadIdx.x, lane = tid & 63;
  const int w = __builtin_amdgcn_readfirstlane(tid >> 6);
  const int panel = (b & 7) * 4 + (b >> 6), slice = (b >> 3) & 7;
  const int pstart = slice < 6 ? 7 * slice : 42 + 6 * (slice - 6);
  const int npairs = slice < 6 ? 7 : 6;
  const int tt = w & 3, kh = w >> 2;
  const int tok = lane & 15, g = lane >> 4;
  STAMP_DECL
  STAMP(0);
  v4u xr[10], yr[10];
  {
    const v4u* yp = (const v4u*)(ws + YF_OFF) + ((size_t)(4 * panel + tt) * KS1 + 10 * kh) * 64 + lane;
    if (FIRST) {
      const u16* xp = gq + (size_t)(64 * panel + 16 * tt + tok) * D + 320 * kh + 8 * g;
#pragma unroll
      for (int i = 0; i < 10; ++i) xr[i] = *(const v4u*)(xp + 32 * i);
    } else {
      const v4u* xp = (const v4u*)(ws + X2F_OFF) + ((size_t)(4 * panel + tt) * KS1 + 10 * kh) * 64 + lane;
#pragma unroll
      for (int i = 0; i < 10; ++i) xr[i] = xp[i * 64];
    }
#pragma unroll
    for (int i = 0; i < 10; ++i) yr[i] = yp[i * 64];
  }
  __builtin_amdgcn_sched_barrier(0);
  const bool active = w < npairs;
  const int P = pstart + (active ? w : 0);
  const v4u* wp = (const v4u*)(ws + W1F_OFF) + (size_t)P * (KS1 * 128) + lane;
  v4u wf[KS1][2];
#pragma unroll
  for (int i = 0; i < G1_PF; ++i) {
    wf[i][0] = wp[(2 * i) * 64];
    wf[i][1] = wp[(2 * i + 1) * 64];
  }
  __builtin_amdgcn_sched_barrier(0);
  STAMP(1);
  LAS f32x2* st = (LAS f32x2*)(smem + G1_A_BYTES);
  part_stats(xr, st + ((0 * 2 + kh) * 4 + tt) * 16 + tok, tok, g);
  __syncthreads();
  STAMP(2);
  {
    const f32x2 s1 = finish_stats(st[((0 * 2 + 0) * 4 + tt) * 16 + tok], st[((0 * 2 + 1) * 4 + tt) * 16 + tok]);
    const f32x2 rs = {s1.y, s1.y}, nm = {-s1.x * s1.y, -s1.x * s1.y};
    LAS v4u* ap = (LAS v4u*)smem + (tt * KS1 + 10 * kh) * 64 + lane;
#pragma unroll
    for (int i = 0; i < 10; ++i) {
#pragma unroll
      for (int c = 0; c < 4; ++c) {
        const f32x2 xv = {lo16(xr[i][c]), hi16(xr[i][c])};
        const f32x2 yv = {lo16(yr[i][c]), hi16(yr[i][c])};
        const f32x2 t = __builtin_elementwise_fma(xv, rs, nm);
        const f32x2 o = __builtin_elementwise_fma(t, yv, xv);
        xr[i][c] = pack2(o.x, o.y);
      }
      ap[i * 64] = xr[i];
    }
  }
  STAMP(3);
  if (w == slice) {
    v4u* xo = (v4u*)(ws + (FIRST ? X1F_OFF : X3F_OFF)) + ((size_t)(4 * panel + tt) * KS1 + 10 * kh) * 64 + lane;
#pragma unroll
    for (int i = 0; i < 10; ++i) xo[i * 64] = xr[i];
  }
  part_stats(xr, st + ((1 * 2 + kh) * 4 + tt) * 16 + tok, tok, g);
  __syncthreads();
  STAMP(4);
#pragma unroll
  for (int i = G1_PF; i < KS1; ++i) {
    wf[i][0] = wp[(2 * i) * 64];
    wf[i][1] = wp[(2 * i + 1) * 64];
  }
  f32x4 cs[2][4];
  __builtin_amdgcn_sched_barrier(0);
  if (!active) return;
  f32x4 acc[2][4];
#pragma unroll
  for (int s = 0; s < 2; ++s)
#pragma unroll
    for (int t = 0; t < 4; ++t) acc[s][t] = f32x4{0.f, 0.f, 0.f, 0.f};
  const LAS v4u* ap = (const LAS v4u*)smem + lane;
#pragma unroll
  for (int i = 0; i < KS1; ++i) {
    if (i == 10) {
      __builtin_amdgcn_sched_barrier(0);
      const f32x4* cp = (const f32x4*)(ws + CS_OFF) + (32 * P + 4 * g) / 4;
#pragma unroll
      for (int kq = 0; kq < 4; ++kq) {
        cs[0][kq] = cp[kq * (F / 4)];
        cs[1][kq] = cp[kq * (F / 4) + 4];
      }
      __builtin_amdgcn_sched_barrier(0);
    }
    v4u a[4];
#pragma unroll
    for (int t = 0; t < 4; ++t) a[t] = ap[(t * KS1 + i) * 64];
#pragma unroll
    for (int t = 0; t < 4; ++t) {
      acc[0][t] = mfma(wf[i][0], a[t], acc[0][t]);
      acc[1][t] = mfma(wf[i][1], a[t], acc[1][t]);
    }
  }
  STAMP(5);
  const f32x4 c0 = (cs[0][0] + cs[0][1]) + (cs[0][2] + cs[0][3]);
  const f32x4 c1 = (cs[1][0] + cs[1][1]) + (cs[1][2] + cs[1][3]);
  v4u* hp = (v4u*)(ws + HF_OFF) + ((size_t)(panel * KS2 + P) * 4) * 64 + lane;
#pragma unroll
  for (int t = 0; t < 4; ++t) {
    const f32x2 s2 = finish_stats(st[((1 * 2 + 0) * 4 + t) * 16 + tok], st[((1 * 2 + 1) * 4 + t) * 16 + tok]);
    float hv[8];
#pragma unroll
    for (int s = 0; s < 2; ++s)
#pragma unroll
      for (int r = 0; r < 4; ++r) {
        const float z = rbf(s2.y * fmaf(-s2.x, s ? c1[r] : c0[r], acc[s][t][r]));
        hv[4 * s + r] = z * __builtin_amdgcn_rcpf(1.f + __expf(-z));
      }
    hp[t * 64] = v4u{pack2(hv[0], hv[1]), pack2(hv[2], hv[3]), pack2(hv[4], hv[5]), pack2(hv[6], hv[7])};
  }
  STAMP(6);
  STAMP_OUT(FIRST ? 1 : 3);
}

constexpr int G2_PF = 6;
constexpr int G2_NK = 14;
constexpr int G2_LDS = 4 * 3 * 5 * 1024;

template <bool LAST>
__global__ __launch_bounds__(256, 1) void k_g2(unsigned char* __restrict__ ws, u16* __restrict__ gout) {
  extern __shared__ __attribute__((aligned(16))) unsigned char smem_raw[];
  LAS unsigned char* smem = (LAS unsigned char*)smem_raw;
  const int b = blockIdx.x, tid = threadIdx.x, lane = tid & 63;
  const int w = __builtin_amdgcn_readfirstlane(tid >> 6);
  const int panel = (b & 7) * 4 + (b >> 6), slice = (b >> 3) & 7;
  const int tok = lane & 15, q = lane >> 4;
  const v4u* hp = (const v4u*)(ws + HF_OFF) + ((size_t)(panel * KS2 + w) * 4) * 64 + lane;
  const v4u* wp = (const v4u*)(ws + W2F_OFF) + ((size_t)(slice * KS2 + w) * 5) * 64 + lane;
  STAMP_DECL
  STAMP(0);
  const bool has_last = w < 2;
  v4u hf[G2_NK][4], wf[G2_NK][5];
#pragma unroll
  for (int i = 0; i < G2_PF; ++i) {
#pragma unroll
    for (int t = 0; t < 4; ++t) hf[i][t] = hp[(i * 16 + t) * 64];
#pragma unroll
    for (int t = 0; t < 5; ++t) wf[i][t] = wp[(i * 20 + t) * 64];
  }
  __builtin_amdgcn_sched_barrier(0);
  v2u xres[5];
  size_t xoff[5];
  {
    const unsigned char* xb = ws + (LAST ? X3F_OFF : X1F_OFF);
#pragma unroll
    for (int t = 0; t < 5; ++t) {
      const int T = 5 * slice + t;
      xoff[t] = ((size_t)(4 * panel + w) * KS1 + (T >> 1)) * 1024 + (tok + 16 * (2 * (T & 1) + (q >> 1))) * 16 + 8 * (q & 1);
      xres[t] = *(const v2u*)(xb + xoff[t]);
    }
  }
  __builtin_amdgcn_sched_barrier(0);
  STAMP(1);
  f32x4 acc[4][5];
#pragma unroll
  for (int a = 0; a < 4; ++a)
#pragma unroll
    for (int t = 0; t < 5; ++t) acc[a][t] = f32x4{0.f, 0.f, 0.f, 0.f};
#pragma unroll
  for (int i = 0; i < G2_NK; ++i) {
    const int j = i + G2_PF;
    if (j < G2_NK) {
      if (j < G2_NK - 1 || has_last) {
#pragma unroll
        for (int t = 0; t < 4; ++t) hf[j][t] = hp[(j * 16 + t) * 64];
#pragma unroll
        for (int t = 0; t < 5; ++t) wf[j][t] = wp[(j * 20 + t) * 64];
      }
    }
    if (i < G2_NK - 1 || has_last) {
#pragma unroll
      for (int t = 0; t < 5; ++t)
#pragma unroll
        for (int a = 0; a < 4; ++a) acc[a][t] = mfma(wf[i][t], hf[i][a], acc[a][t]);
    }
    __builtin_amdgcn_sched_barrier(0);
  }
  STAMP(2);
  LAS f32x4* red = (LAS f32x4*)smem;
#pragma unroll
  for (int a = 0; a < 4; ++a) {
    if (a != w) {
      const int slot = (w - a - 1) & 3;
#pragma unroll
      for (int t = 0; t < 5; ++t) red[((a * 3 + slot) * 5 + t) * 64 + lane] = acc[a][t];
    }
  }
  __syncthreads();
  STAMP(3);
  f32x4 fin[5];
#pragma unroll
  for (int t = 0; t < 5; ++t) {
    fin[t] = w == 0 ? acc[0][t] : (w == 1 ? acc[1][t] : (w == 2 ? acc[2][t] : acc[3][t]));
#pragma unroll
    for (int s = 0; s < 3; ++s) fin[t] += red[((w * 3 + s) * 5 + t) * 64 + lane];
  }
#pragma unroll
  for (int t = 0; t < 5; ++t) {
    const float o0 = lo16(xres[t].x) + rbf(fin[t][0]);
    const float o1 = hi16(xres[t].x) + rbf(fin[t][1]);
    const float o2 = lo16(xres[t].y) + rbf(fin[t][2]);
    const float o3 = hi16(xres[t].y) + rbf(fin[t][3]);
    const v2u o = v2u{pack2(o0, o1), pack2(o2, o3)};
    if (LAST) {
      *(v2u*)(gout + (size_t)(64 * panel + 16 * w + tok) * D + 80 * slice + 16 * t + 4 * q) = o;
    } else {
      *(v2u*)(ws + X2F_OFF + xoff[t]) = o;
    }
  }
  STAMP(6);
  STAMP_OUT(LAST ? 4 : 2);
}

}

extern "C" void kernel_launch(void* const* d_in, const int* in_sizes, int n_in, void* d_out, int out_size, void* d_ws,
                              size_t ws_size, hipStream_t stream) {
  const u16* q = (const u16*)d_in[0];
  const u16* k = (const u16*)d_in[1];
  const u16* v = (const u16*)d_in[2];
  const u16* shift_k = (const u16*)d_in[3];
  const u16* ssm_k = (const u16*)d_in[4];
  const u16* ssm_D = (const u16*)d_in[5];
  const u16* w1 = (const u16*)d_in[7];
  const u16* w2 = (const u16*)d_in[8];
  unsigned char* ws = (unsigned char*)d_ws;
  u16* out = (u16*)d_out;
  (void)hipFuncSetAttribute((const void*)k_conv_prep, hipFuncAttributeMaxDynamicSharedMemorySize, K1_LDS);
  (void)hipFuncSetAttribute((const void*)k_g1<true>, hipFuncAttributeMaxDynamicSharedMemorySize, G1_LDS);
  (void)hipFuncSetAttribute((const void*)k_g1<false>, hipFuncAttributeMaxDynamicSharedMemorySize, G1_LDS);
  (void)hipFuncSetAttribute((const void*)k_g2<true>, hipFuncAttributeMaxDynamicSharedMemorySize, G2_LDS);
  (void)hipFuncSetAttribute((const void*)k_g2<false>, hipFuncAttributeMaxDynamicSharedMemorySize, G2_LDS);
  k_kv<<<320, 256, 0, stream>>>(k, v, shift_k, ws);
  k_conv_prep<<<CONV_WGS + PREP_WGS, 256, K1_LDS, stream>>>(ssm_k, ssm_D, w1, w2, ws);
  k_g1<true><<<256, 512, G1_LDS, stream>>>(q, ws);
  k_g2<false><<<256, 256, G2_LDS, stream>>>(ws, out);
  k_g1<false><<<256, 512, G1_LDS, stream>>>(q, ws);
  k_g2<true><<<256, 256, G2_LDS, stream>>>(ws, out);
}
```

```cpp
#include <hip/hip_runtime.h>
#include <hip/hip_bf16.h>
#include <stdint.h>

#define GAS __attribute__((address_space(1)))
#define LAS __attribute__((address_space(3)))
typedef unsigned v4u __attribute__((ext_vector_type(4)));
typedef unsigned v2u __attribute__((ext_vector_type(2)));
typedef float f32x4 __attribute__((ext_vector_type(4)));
typedef float f32x2 __attribute__((ext_vector_type(2)));
typedef short bf16x8 __attribute__((ext_vector_type(8)));
typedef short s16x4 __attribute__((ext_vector_type(4)));
typedef __bf16 bf16x2_t __attribute__((ext_vector_type(2)));
typedef unsigned short u16;

#define STAMP(n)
#define STAMP_DECL
#define STAMP_OUT(kid)

namespace {

constexpr int S = 2048, D = 640, F = 1728;
constexpr int KS1 = D / 32;
constexpr int KS2 = F / 32;
constexpr size_t WF_BYTES = (size_t)D * F * 2;
constexpr size_t XF_BYTES = (size_t)S * D * 2;
constexpr size_t HF_BYTES = (size_t)S * F * 2;
constexpr size_t W1F_OFF = 0;
constexpr size_t W2F_OFF = W1F_OFF + WF_BYTES;
constexpr size_t YF_OFF = W2F_OFF + WF_BYTES;
constexpr size_t X1F_OFF = YF_OFF + XF_BYTES;
constexpr size_t X2F_OFF = X1F_OFF + XF_BYTES;
constexpr size_t X3F_OFF = X2F_OFF + XF_BYTES;
constexpr size_t HF_OFF = X3F_OFF + XF_BYTES;
constexpr size_t KVT_OFF = HF_OFF + HF_BYTES;
constexpr size_t CS_OFF = KVT_OFF + XF_BYTES;

#define MAP84 1
#define PANEL_OF(b) ((((b) & 7) >> 1) * 8 + ((b) >> 5))
#define SLICE_OF(b) (((b) & 1) * 4 + (((b) >> 3) & 3))
__device__ __forceinline__ unsigned pack2(float a, float b) {
  f32x2 v = {a, b};
  bf16x2_t r = __builtin_convertvector(v, bf16x2_t);
  return __builtin_bit_cast(unsigned, r);
}
__device__ __forceinline__ float lo16(unsigned u) { return __builtin_bit_cast(float, u << 16); }
__device__ __forceinline__ float hi16(unsigned u) { return __builtin_bit_cast(float, u & 0xffff0000u); }
__device__ __forceinline__ float rbf(float a) { return lo16(pack2(a, a)); }
__device__ __forceinline__ float bf2f(u16 b) { return __builtin_bit_cast(float, ((unsigned)b) << 16); }
__device__ __forceinline__ f32x4 mfma(v4u a, v4u b, f32x4 c) {
  return __builtin_amdgcn_mfma_f32_16x16x32_bf16(__builtin_bit_cast(bf16x8, a), __builtin_bit_cast(bf16x8, b), c, 0, 0, 0);
}
__device__ __forceinline__ v2u tr_read(LAS unsigned char* p) {
  return __builtin_bit_cast(v2u, __builtin_amdgcn_ds_read_tr16_b64_v4i16((LAS s16x4*)p));
}

constexpr int K0_RSTR = 144;
__global__ __launch_bounds__(256) void k_kv(const u16* __restrict__ gk, const u16* __restrict__ gv,
                                            const u16* __restrict__ gshift, unsigned char* __restrict__ ws) {
  __shared__ __attribute__((aligned(16))) unsigned char tile[64 * K0_RSTR];
  const int b = blockIdx.x, tid = threadIdx.x;
  const int t0 = (b & 31) * 64, d0 = (b >> 5) * 64;
  const int tr = tid >> 3, c8 = tid & 7;
  const int tb = t0 + 2 * tr, dc = d0 + 8 * c8;
  v4u kr[5], vr[2], sw[4];
#pragma unroll
  for (int r = 0; r < 5; ++r) {
    const int t = tb - 3 + r;
    kr[r] = *(const v4u*)(gk + (size_t)(t < 0 ? 0 : t) * D + dc);
  }
#pragma unroll
  for (int r = 0; r < 2; ++r) vr[r] = *(const v4u*)(gv + (size_t)(tb + r) * D + dc);
#pragma unroll
  for (int r = 0; r < 4; ++r) sw[r] = *(const v4u*)(gshift + dc * 4 + 8 * r);
#pragma unroll
  for (int r = 0; r < 3; ++r)
    if (tb - 3 + r < 0) kr[r] = v4u{0u, 0u, 0u, 0u};
#pragma unroll
  for (int j = 0; j < 8; ++j) {
    const unsigned wa = sw[j >> 1][2 * (j & 1)], wb = sw[j >> 1][2 * (j & 1) + 1];
    const float w0 = lo16(wa), w1 = hi16(wa), w2 = lo16(wb), w3 = hi16(wb);
    float kf[5];
#pragma unroll
    for (int r = 0; r < 5; ++r) kf[r] = (j & 1) ? hi16(kr[r][j >> 1]) : lo16(kr[r][j >> 1]);
    const float v0 = (j & 1) ? hi16(vr[0][j >> 1]) : lo16(vr[0][j >> 1]);
    const float v1 = (j & 1) ? hi16(vr[1][j >> 1]) : lo16(vr[1][j >> 1]);
    const float ks0 = fmaf(kf[3], w0, fmaf(kf[2], w1, fmaf(kf[1], w2, kf[0] * w3)));
    const float ks1 = fmaf(kf[4], w0, fmaf(kf[3], w1, fmaf(kf[2], w2, kf[1] * w3)));
    const unsigned kp = pack2(ks0, ks1);
    *(unsigned*)(tile + (8 * c8 + j) * K0_RSTR + 124 - 4 * tr) = pack2(hi16(kp) * v1, lo16(kp) * v0);
  }
  __syncthreads();
  const int ch = tid >> 2, part = tid & 3;
  const v4u o0 = *(const v4u*)(tile + ch * K0_RSTR + part * 32);
  const v4u o1 = *(const v4u*)(tile + ch * K0_RSTR + part * 32 + 16);
  v4u* dst = (v4u*)(ws + KVT_OFF + ((size_t)(d0 + ch) * S + (1984 - t0) + 16 * part) * 2);
  dst[0] = o0;
  dst[1] = o1;
}

constexpr int CONV_WGS = 160, PREP_WGS = 96;
constexpr int XR_BYTES = 4608;
constexpr int RS = 4160;
constexpr int CH_BYTES = XR_BYTES + 4 * RS;
constexpr int K1_LDS = 4 * 5 * 32 * 144;

#define WAVE_SYNC()                                      \
  do {                                                   \
    __builtin_amdgcn_fence(__ATOMIC_RELEASE, "wavefront"); \
    __builtin_amdgcn_wave_barrier();                     \
    __builtin_amdgcn_fence(__ATOMIC_ACQUIRE, "wavefront"); \
  } while (0)

__global__ __launch_bounds__(256, 1) void k_conv_prep(const u16* __restrict__ gssm, const u16* __restrict__ gD,
                                                      const u16* __restrict__ gw1, const u16* __restrict__ gw2,
                                                      unsigned char* __restrict__ ws) {
  extern __shared__ __attribute__((aligned(16))) unsigned char smem_raw[];
  LAS unsigned char* smem = (LAS unsigned char*)smem_raw;
  const int b = blockIdx.x, tid = threadIdx.x, lane = tid & 63;
  const int w = __builtin_amdgcn_readfirstlane(tid >> 6);
  STAMP_DECL
  STAMP(0);
  if (b < CONV_WGS) {
    const int cg = (b & 7) * 20 + (b >> 3), d0 = 4 * cg;
    const u16* hrow = gssm + (size_t)(d0 + w) * S;
    const v4u* xrow = (const v4u*)(ws + KVT_OFF + (size_t)(d0 + w) * S * 2);
    v4u ha[4], xa[4];
    v2u hn[4];
#pragma unroll
    for (int u = 0; u < 4; ++u) {
      const int c = 64 * u + lane;
      ha[u] = *(const v4u*)(hrow + 8 * c);
      hn[u] = *(const v2u*)(hrow + 8 * (c < 255 ? c + 1 : c));
    }
#pragma unroll
    for (int u = 0; u < 4; ++u) xa[u] = xrow[64 * u + lane];
    const unsigned Dbits = gD[d0 + w];
    __builtin_amdgcn_sched_barrier(0);
    STAMP(1);
    if (lane == 63) hn[3] = v2u{0u, 0u};

    LAS unsigned char* chb = smem + w * CH_BYTES;
    LAS unsigned char* hs = chb + XR_BYTES;
#pragma unroll
    for (int u = 0; u < 4; ++u) {
      const int c = 64 * u + lane;
      const v4u a = ha[u];
      const v2u n = hn[u];
      const unsigned s01 = __builtin_amdgcn_alignbyte(a.y, a.x, 2), s12 = __builtin_amdgcn_alignbyte(a.z, a.y, 2),
                     s23 = __builtin_amdgcn_alignbyte(a.w, a.z, 2), s34 = __builtin_amdgcn_alignbyte(n.x, a.w, 2),
                     s45 = __builtin_amdgcn_alignbyte(n.y, n.x, 2);
      LAS unsigned char* dst = hs + (c + 2) * 16;
      *(LAS v4u*)(dst) = a;
      *(LAS v4u*)(dst + RS) = v4u{s01, s12, s23, s34};
      *(LAS v4u*)(dst + 2 * RS) = v4u{a.y, a.z, a.w, n.x};
      *(LAS v4u*)(dst + 3 * RS) = v4u{s12, s23, s34, s45};
    }
    {
      const unsigned f0 = __builtin_amdgcn_readfirstlane(ha[0].x), f1 = __builtin_amdgcn_readfirstlane(ha[0].y);
      if (lane < 2) {
        const unsigned n0 = lane ? f0 : 0u, n1 = lane ? f1 : 0u;
        LAS unsigned char* dst = hs + lane * 16;
        *(LAS v4u*)(dst) = v4u{0u, 0u, 0u, 0u};
        *(LAS v4u*)(dst + RS) = v4u{0u, 0u, 0u, n0 << 16};
        *(LAS v4u*)(dst + 2 * RS) = v4u{0u, 0u, 0u, n0};
        *(LAS v4u*)(dst + 3 * RS) = v4u{0u, 0u, n0 << 16, __builtin_amdgcn_alignbyte(n1, n0, 2)};
      }
      if (lane < 30) *(LAS v4u*)(chb + 4096 + lane * 16) = v4u{0u, 0u, 0u, 0u};
    }
#pragma unroll
    for (int u = 0; u < 4; ++u) *(LAS v4u*)(chb + (64 * u + lane) * 16) = xa[u];
    WAVE_SYNC();
    STAMP(2);
    const int i16 = lane & 15, q = lane >> 4;
    const int sig = (1 + i16) & 3;
    LAS unsigned char* abase = hs + sig * RS + 2 * (1 + i16 - sig) + 16 * q;
    LAS unsigned char* bbase = chb + 480 - 32 * i16 + 16 * q;
    v4u B[2][8];
#pragma unroll
    for (int ww = 0; ww < 8; ++ww) B[0][ww] = *(LAS v4u*)(bbase + 64 * (56 + ww));
    v4u A[64];
#pragma unroll
    for (int c = 0; c < 64; ++c) {
      const v2u lo = *(volatile LAS v2u*)(abase + 64 * c);
      const v2u hi = *(volatile LAS v2u*)(abase + 64 * c + 8);
      A[c] = v4u{lo.x, lo.y, hi.x, hi.y};
    }
    v4u Ad;
    {
      const int j = 15 - i16 - 8 * q;
      const unsigned dv = (j & 1) ? (Dbits << 16) : Dbits;
#pragma unroll
      for (int c = 0; c < 4; ++c) Ad[c] = (j >= 0 && (j >> 1) == c) ? dv : 0u;
    }
    f32x4 acc[8];
#pragma unroll
    for (int n = 0; n < 8; ++n) acc[n] = f32x4{0.f, 0.f, 0.f, 0.f};
    STAMP(3);
#pragma unroll
    for (int v = 0; v < 8; ++v) {
      if (v < 7) {
#pragma unroll
        for (int ww = 0; ww < 8; ++ww) B[(v + 1) & 1][ww] = *(LAS v4u*)(bbase + 64 * (56 - (8 * (v + 1) - ww)));
      }
      __builtin_amdgcn_sched_barrier(0);
      acc[v] = mfma(Ad, B[v & 1][0], acc[v]);
#pragma unroll
      for (int u = 0; u < 8; ++u) {
        if (u + v < 8) {
#pragma unroll
          for (int ww = 0; ww < 8; ++ww) acc[u + v] = mfma(A[8 * u + ww], B[v & 1][ww], acc[u + v]);
        }
      }
      __builtin_amdgcn_sched_barrier(0);
    }
    STAMP(5);
    LAS unsigned char* yt = hs + 64 * w;
#pragma unroll
    for (int n = 0; n < 8; ++n) {
      const int s0 = 256 * n + 16 * i16 + 4 * q;
      *(LAS v2u*)(yt + s0 * 2) = v2u{pack2(acc[n][0], acc[n][1]), pack2(acc[n][2], acc[n][3])};
    }
    __syncthreads();
    {
      const int qq = (lane & 15) >> 2, pp = lane & 3;
      LAS unsigned char* tb = smem + qq * CH_BYTES + XR_BYTES + 64 * qq + (16 * q + 4 * pp) * 2;
      unsigned char* yf = ws + YF_OFF + (size_t)(d0 >> 5) * 1024 + ((d0 >> 3) & 3) * 256 + ((d0 >> 2) & 1) * 8 + (lane & 15) * 16;
#pragma unroll
      for (int it = 0; it < 8; ++it) {
        const int tok0 = (w * 8 + it) * 64;
        const v2u r = tr_read(tb + tok0 * 2);
        const int tt = (tok0 >> 4) + q;
        *(v2u*)(yf + (size_t)tt * (KS1 * 1024)) = r;
      }
    }
    STAMP(6);
    STAMP_OUT(0);
  } else {
    const int gw = (b - CONV_WGS) * 4 + w;
    LAS unsigned char* tl = smem + w * (5 * 32 * 144);
    const int r8 = lane >> 3, c8 = lane & 7;
    const int g = lane >> 4, qq = (lane & 15) >> 2, pp = lane & 3;
    if (gw < 108) {
      const int nb = gw % 27, kq = gw / 27;
      v4u ld[5][4];
#pragma unroll
      for (int r = 0; r < 5; ++r) {
        const u16* src = gw1 + (size_t)(5 * kq + r) * 32 * F + nb * 64;
#pragma unroll
        for (int u = 0; u < 4; ++u) ld[r][u] = *(const v4u*)(src + (size_t)(8 * u + r8) * F + c8 * 8);
      }
      f32x4 cs[4];
#pragma unroll
      for (int t = 0; t < 4; ++t) cs[t] = f32x4{0.f, 0.f, 0.f, 0.f};
      const v4u ones = v4u{0x3f803f80u, 0x3f803f80u, 0x3f803f80u, 0x3f803f80u};
#pragma unroll
      for (int r = 0; r < 5; ++r) {
        LAS unsigned char* tr = tl + r * (32 * 144);
#pragma unroll
        for (int u = 0; u < 4; ++u) *(LAS v4u*)(tr + (8 * u + r8) * 144 + c8 * 16) = ld[r][u];
        WAVE_SYNC();
        const int i = 5 * kq + r;
#pragma unroll
        for (int t = 0; t < 4; ++t) {
          const v2u lo = tr_read(tr + (8 * g + qq) * 144 + (16 * t + 4 * pp) * 2);
          const v2u hi = tr_read(tr + (8 * g + 4 + qq) * 144 + (16 * t + 4 * pp) * 2);
          const int T = 4 * nb + t;
          const v4u f = v4u{lo.x, lo.y, hi.x, hi.y};
          *(v4u*)(ws + W1F_OFF + (size_t)(((T >> 1) * KS1 + i) * 2 + (T & 1)) * 1024 + lane * 16) = f;
          cs[t] = mfma(f, ones, cs[t]);
        }
      }
      if ((lane & 15) == 0) {
#pragma unroll
        for (int t = 0; t < 4; ++t) *(f32x4*)(ws + CS_OFF + ((size_t)kq * F + nb * 64 + 16 * t + 4 * g) * 4) = cs[t];
      }
    } else if (gw < 378) {
      v4u ld[2][4];
#pragma unroll
      for (int r = 0; r < 2; ++r) {
        const int id = (gw - 108) * 2 + r;
        const u16* src = gw2 + (size_t)(id / 10) * 32 * D + (id % 10) * 64;
#pragma unroll
        for (int u = 0; u < 4; ++u) ld[r][u] = *(const v4u*)(src + (size_t)(8 * u + r8) * D + c8 * 8);
      }
#pragma unroll
      for (int r = 0; r < 2; ++r) {
        const int id = (gw - 108) * 2 + r;
        LAS unsigned char* tr = tl + r * (32 * 144);
#pragma unroll
        for (int u = 0; u < 4; ++u) *(LAS v4u*)(tr + (8 * u + r8) * 144 + c8 * 16) = ld[r][u];
        WAVE_SYNC();
        const int i = id / 10, db = id % 10;
#pragma unroll
        for (int t = 0; t < 4; ++t) {
          const v2u lo = tr_read(tr + (4 * g + qq) * 144 + (16 * t + 4 * pp) * 2);
          const v2u hi = tr_read(tr + (16 + 4 * g + qq) * 144 + (16 * t + 4 * pp) * 2);
          const int T = 4 * db + t;
          *(v4u*)(ws + W2F_OFF + (size_t)(((T / 5) * KS2 + i) * 5 + (T % 5)) * 1024 + lane * 16) = v4u{lo.x, lo.y, hi.x, hi.y};
        }
      }
    }
    STAMP(6);
    STAMP_OUT(0);
  }
}

#define G1_NG 20
#define G1_NL 2
#define NT_OUT 0
#define ST_OUT(v, p) (*(p) = (v))
constexpr int G1_A_BYTES = 4 * KS1 * 1024;
constexpr int G1_ST_OFF = G1_A_BYTES;
constexpr int G1_LDS = G1_ST_OFF + 2048;

__device__ __forceinline__ f32x2 finish_stats(float sum, float ssq) {
  const float mean = sum * (1.f / 640.f);
  const float var = fmaxf(ssq * (1.f / 640.f) - mean * mean, 0.f);
  const float mb = rbf(mean);
  const float sd = rbf(__builtin_amdgcn_sqrtf(rbf(rbf(var) + 1e-5f)));
  const float rs = __builtin_amdgcn_rcpf(sd);
  return f32x2{rs, -mb * rs};
}
__device__ __forceinline__ float gram_diag(f32x4 gx, int tok) {
  const int r = tok & 3;
  const float ss = r == 0 ? gx[0] : (r == 1 ? gx[1] : (r == 2 ? gx[2] : gx[3]));
  return __shfl(ss, tok + 16 * (tok >> 2));
}

#define LDS_BARRIER()                                               \
  do {                                                              \
    __builtin_amdgcn_fence(__ATOMIC_RELEASE, "workgroup", "local"); \
    __builtin_amdgcn_s_barrier();                                   \
    __builtin_amdgcn_fence(__ATOMIC_ACQUIRE, "workgroup", "local"); \
  } while (0)

__device__ __forceinline__ void g1_colsum(const unsigned char* ws, int P, int g, f32x4& c0, f32x4& c1) {
  const f32x4* cp = (const f32x4*)(ws + CS_OFF) + (32 * P + 4 * g) / 4;
  c0 = (cp[0] + cp[F / 4]) + (cp[2 * (F / 4)] + cp[3 * (F / 4)]);
  c1 = (cp[4] + cp[F / 4 + 4]) + (cp[2 * (F / 4) + 4] + cp[3 * (F / 4) + 4]);
}
__device__ __forceinline__ v4u g1_silu(f32x4 a0, f32x4 a1, f32x2 st, f32x4 c0, f32x4 c1) {
  v4u o;
  const f32x4 rs = {st.x, st.x, st.x, st.x};
#pragma unroll
  for (int s = 0; s < 2; ++s) {
    const f32x4 z = __builtin_elementwise_fma(s ? a1 : a0, rs, (s ? c1 : c0) * st.y);
    const f32x4 zl = z * (-1.44269504f);
    f32x4 e;
#pragma unroll
    for (int r = 0; r < 4; ++r) e[r] = __builtin_amdgcn_exp2f(zl[r]);
    e = e + 1.f;
#pragma unroll
    for (int r = 0; r < 4; ++r) e[r] = __builtin_amdgcn_rcpf(e[r]);
    const f32x4 h = z * e;
    o[2 * s] = pack2(h[0], h[1]);
    o[2 * s + 1] = pack2(h[2], h[3]);
  }
  return o;
}

template <bool FIRST, bool ACTIVE>
__device__ __forceinline__ void g1_body(const u16* __restrict__ gq, unsigned char* __restrict__ ws, LAS unsigned char* smem,
                                        int panel, int slice, int P, int w, int lane) {
  const int tt = w & 3, kh = w >> 2;
  const int tok = lane & 15, g = lane >> 4;
  STAMP_DECL
  STAMP(0);
  v4u xr[10], yr[10];
  if (FIRST) {
    const u16* xp = gq + (size_t)(64 * panel + 16 * tt + tok) * D + 320 * kh + 8 * g;
#pragma unroll
    for (int i = 0; i < 10; ++i) xr[i] = *(const v4u*)(xp + 32 * i);
  } else {
    const v4u* xp = (const v4u*)(ws + X2F_OFF) + ((size_t)(4 * panel + tt) * KS1 + 10 * kh) * 64 + lane;
#pragma unroll
    for (int i = 0; i < 10; ++i) xr[i] = xp[i * 64];
  }
  __builtin_amdgcn_sched_barrier(0);
  __builtin_amdgcn_s_barrier();
  __builtin_amdgcn_sched_barrier(0);
  {
    const v4u* yp = (const v4u*)(ws + YF_OFF) + ((size_t)(4 * panel + tt) * KS1 + 10 * kh) * 64 + lane;
#pragma unroll
    for (int i = 0; i < 10; ++i) yr[i] = __builtin_nontemporal_load(yp + i * 64);
  }
  __builtin_amdgcn_sched_barrier(0);
  STAMP(1);
  const v4u* wp = (const v4u*)(ws + W1F_OFF) + (size_t)P * (KS1 * 128) + lane;
  v4u wf[40];
#define G1_WLOAD(f) do { if (ACTIVE && (f) < 40) wf[f] = wp[(f) * 64]; } while (0)
  const v4u ones = v4u{0x3f803f80u, 0x3f803f80u, 0x3f803f80u, 0x3f803f80u};
  LAS f32x2* st = (LAS f32x2*)(smem + G1_ST_OFF);
  {
    f32x4 sa = {0.f, 0.f, 0.f, 0.f}, ga = sa, sb = sa, gb = sa;
#pragma unroll
    for (int i = 0; i < 10; i += 2) {
      sa = mfma(ones, xr[i], sa);
      ga = mfma(xr[i], xr[i], ga);
      sb = mfma(ones, xr[i + 1], sb);
      gb = mfma(xr[i + 1], xr[i + 1], gb);
    }
    ga += gb;
    const int r = tok & 3;
    const float ss = r == 0 ? ga[0] : (r == 1 ? ga[1] : (r == 2 ? ga[2] : ga[3]));
    if (g == (tok >> 2)) st[((0 * 2 + kh) * 4 + tt) * 16 + tok] = f32x2{sa[0] + sb[0], ss};
  }
  __builtin_amdgcn_sched_barrier(0);
  LDS_BARRIER();
  STAMP(2);
  {
    const f32x2 pa = st[((0 * 2 + 0) * 4 + tt) * 16 + tok], pb = st[((0 * 2 + 1) * 4 + tt) * 16 + tok];
    const f32x2 s1 = finish_stats(pa.x + pb.x, pa.y + pb.y);
    const f32x2 rs = {s1.x, s1.x}, nm = {s1.y, s1.y};
    LAS v4u* awp = (LAS v4u*)smem + (tt * KS1 + 10 * kh) * 64 + lane;
    f32x4 sx = {0.f, 0.f, 0.f, 0.f}, gx = sx;
#pragma unroll
    for (int i = 0; i < 10; ++i) {
#pragma unroll
      for (int f = (G1_NG * i) / 10; f < (G1_NG * (i + 1)) / 10; ++f) G1_WLOAD(f);
#pragma unroll
      for (int c = 0; c < 4; ++c) {
        const f32x2 xv = {lo16(xr[i][c]), hi16(xr[i][c])};
        const f32x2 yv = {lo16(yr[i][c]), hi16(yr[i][c])};
        const f32x2 t = __builtin_elementwise_fma(xv, rs, nm);
        const f32x2 o = __builtin_elementwise_fma(t, yv, xv);
        xr[i][c] = pack2(o.x, o.y);
      }
      awp[i * 64] = xr[i];
      sx = mfma(ones, xr[i], sx);
      gx = mfma(xr[i], xr[i], gx);
      __builtin_amdgcn_sched_barrier(0);
    }
    const int r = tok & 3;
    const float ss = r == 0 ? gx[0] : (r == 1 ? gx[1] : (r == 2 ? gx[2] : gx[3]));
    if (g == (tok >> 2)) st[((1 * 2 + kh) * 4 + tt) * 16 + tok] = f32x2{sx[0], ss};
  }
  STAMP(3);
  LDS_BARRIER();
  STAMP(4);
  if (!ACTIVE) {
    if (w == 7) {
      const LAS v4u* src = (const LAS v4u*)smem + (10 * slice) * 64 + lane;
      v4u* xo = (v4u*)(ws + (FIRST ? X1F_OFF : X3F_OFF)) + ((size_t)(4 * panel) * KS1 + 10 * slice) * 64 + lane;
#pragma unroll
      for (int i = 0; i < 10; ++i) ST_OUT(src[i * 64], xo + i * 64);
    }
    return;
  }
  f32x4 acc[2][4];
#pragma unroll
  for (int s = 0; s < 2; ++s)
#pragma unroll
    for (int t = 0; t < 4; ++t) acc[s][t] = f32x4{0.f, 0.f, 0.f, 0.f};
  const LAS v4u* ap = (const LAS v4u*)smem + lane;
  v4u a[2][4];
#pragma unroll
  for (int t = 0; t < 4; ++t) a[0][t] = ap[(t * KS1) * 64];
  f32x4 cs[8];
  {
    const f32x4* cp = (const f32x4*)(ws + CS_OFF) + (32 * P + 4 * g) / 4;
#pragma unroll
    for (int kq = 0; kq < 4; ++kq) {
      cs[2 * kq] = cp[kq * (F / 4)];
      cs[2 * kq + 1] = cp[kq * (F / 4) + 4];
    }
  }
  f32x2 sp[8];
#pragma unroll
  for (int t = 0; t < 8; ++t) sp[t] = st[(2 * 4 + t) * 16 + tok];
  __builtin_amdgcn_sched_barrier(0);
#pragma unroll
  for (int i = 0; i < KS1; ++i) {
    if (i + 1 < KS1) {
#pragma unroll
      for (int t = 0; t < 4; ++t) a[(i + 1) & 1][t] = ap[(t * KS1 + i + 1) * 64];
    }
#pragma unroll
    for (int f = G1_NG + G1_NL * i; f < G1_NG + G1_NL * (i + 1); ++f) G1_WLOAD(f);
#pragma unroll
    for (int t = 0; t < 4; ++t) {
      acc[0][t] = mfma(wf[2 * i], a[i & 1][t], acc[0][t]);
      acc[1][t] = mfma(wf[2 * i + 1], a[i & 1][t], acc[1][t]);
    }
#pragma unroll
    for (int r = 0; r < 4; ++r) {
      __builtin_amdgcn_sched_group_barrier(0x8, 1, 0);
      __builtin_amdgcn_sched_group_barrier(0x100, 1, 0);
      __builtin_amdgcn_sched_group_barrier(0x8, 1, 0);
      __builtin_amdgcn_sched_group_barrier(0x20, 1, 0);
    }
    __builtin_amdgcn_sched_barrier(0);
  }
  STAMP(5);
  const f32x4 c0 = (cs[0] + cs[2]) + (cs[4] + cs[6]), c1 = (cs[1] + cs[3]) + (cs[5] + cs[7]);
  v4u* hp = (v4u*)(ws + HF_OFF) + ((size_t)(panel * KS2 + P) * 4) * 64 + lane;
#pragma unroll
  for (int t = 0; t < 4; ++t) {
    const f32x2 s2 = finish_stats(sp[t].x + sp[4 + t].x, sp[t].y + sp[4 + t].y);
    ST_OUT(g1_silu(acc[0][t], acc[1][t], s2, c0, c1), hp + t * 64);
  }
  STAMP(6);
  STAMP_OUT(FIRST ? 1 : 3);
}

template <bool FIRST>
__global__ __launch_bounds__(512, 2) void k_g1(const u16* __restrict__ gq, unsigned char* __restrict__ ws) {
  extern __shared__ __attribute__((aligned(16))) unsigned char smem_raw[];
  LAS unsigned char* smem = (LAS unsigned char*)smem_raw;
  const int b = blockIdx.x, tid = threadIdx.x, lane = tid & 63;
  const int w = __builtin_amdgcn_readfirstlane(tid >> 6);
  const int panel = PANEL_OF(b), slice = SLICE_OF(b);
  const int pstart = slice < 6 ? 7 * slice : 42 + 6 * (slice - 6);
  const int npairs = slice < 6 ? 7 : 6;
  if (w < npairs)
    g1_body<FIRST, true>(gq, ws, smem, panel, slice, pstart + w, w, lane);
  else
    g1_body<FIRST, false>(gq, ws, smem, panel, slice, pstart, w, lane);
}

constexpr int G2_PF = 6;
constexpr int G2_NK = 14;
constexpr int G2_LDS = 4 * 3 * 5 * 1024;

template <bool LAST>
__global__ __launch_bounds__(256, 1) void k_g2(unsigned char* __restrict__ ws, u16* __restrict__ gout) {
  extern __shared__ __attribute__((aligned(16))) unsigned char smem_raw[];
  LAS unsigned char* smem = (LAS unsigned char*)smem_raw;
  const int b = blockIdx.x, tid = threadIdx.x, lane = tid & 63;
  const int w = __builtin_amdgcn_readfirstlane(tid >> 6);
  const int panel = PANEL_OF(b), slice = SLICE_OF(b);
  const int tok = lane & 15, q = lane >> 4;
  const v4u* hp = (const v4u*)(ws + HF_OFF) + ((size_t)(panel * KS2 + w) * 4) * 64 + lane;
  const v4u* wp = (const v4u*)(ws + W2F_OFF) + ((size_t)(slice * KS2 + w) * 5) * 64 + lane;
  STAMP_DECL
  STAMP(0);
  const bool has_last = w < 2;
  v4u hf[G2_NK][4], wf[G2_NK][5];
#pragma unroll
  for (int i = 0; i < G2_PF; ++i) {
#pragma unroll
    for (int t = 0; t < 4; ++t) hf[i][t] = hp[(i * 16 + t) * 64];
#pragma unroll
    for (int t = 0; t < 5; ++t) wf[i][t] = wp[(i * 20 + t) * 64];
  }
  __builtin_amdgcn_sched_barrier(0);
  v2u xres[5];
  size_t xoff[5];
  {
    const unsigned char* xb = ws + (LAST ? X3F_OFF : X1F_OFF);
#pragma unroll
    for (int t = 0; t < 5; ++t) {
      const int T = 5 * slice + t;
      xoff[t] = ((size_t)(4 * panel + w) * KS1 + (T >> 1)) * 1024 + (tok + 16 * (2 * (T & 1) + (q >> 1))) * 16 + 8 * (q & 1);
      xres[t] = *(const v2u*)(xb + xoff[t]);
    }
  }
  __builtin_amdgcn_sched_barrier(0);
  STAMP(1);
  f32x4 acc[4][5];
#pragma unroll
  for (int a = 0; a < 4; ++a)
#pragma unroll
    for (int t = 0; t < 5; ++t) acc[a][t] = f32x4{0.f, 0.f, 0.f, 0.f};
#pragma unroll
  for (int i = 0; i < G2_NK; ++i) {
    const int j = i + G2_PF;
    if (j < G2_NK) {
      if (j < G2_NK - 1 || has_last) {
#pragma unroll
        for (int t = 0; t < 4; ++t) hf[j][t] = hp[(j * 16 + t) * 64];
#pragma unroll
        for (int t = 0; t < 5; ++t) wf[j][t] = wp[(j * 20 + t) * 64];
      }
    }
    if (i < G2_NK - 1 || has_last) {
#pragma unroll
      for (int t = 0; t < 5; ++t)
#pragma unroll
        for (int a = 0; a < 4; ++a) acc[a][t] = mfma(wf[i][t], hf[i][a], acc[a][t]);
    }
    __builtin_amdgcn_sched_barrier(0);
  }
  STAMP(2);
  LAS f32x4* red = (LAS f32x4*)smem;
#pragma unroll
  for (int a = 0; a < 4; ++a) {
    if (a != w) {
      const int slot = (w - a - 1) & 3;
#pragma unroll
      for (int t = 0; t < 5; ++t) red[((a * 3 + slot) * 5 + t) * 64 + lane] = acc[a][t];
    }
  }
  __syncthreads();
  STAMP(3);
  f32x4 fin[5];
#pragma unroll
  for (int t = 0; t < 5; ++t) {
    fin[t] = w == 0 ? acc[0][t] : (w == 1 ? acc[1][t] : (w == 2 ? acc[2][t] : acc[3][t]));
#pragma unroll
    for (int s = 0; s < 3; ++s) fin[t] += red[((w * 3 + s) * 5 + t) * 64 + lane];
  }
#pragma unroll
  for (int t = 0; t < 5; ++t) {
    const float o0 = lo16(xres[t].x) + rbf(fin[t][0]);
    const float o1 = hi16(xres[t].x) + rbf(fin[t][1]);
    const float o2 = lo16(xres[t].y) + rbf(fin[t][2]);
    const float o3 = hi16(xres[t].y) + rbf(fin[t][3]);
    const v2u o = v2u{pack2(o0, o1), pack2(o2, o3)};
    if (LAST) {
      *(v2u*)(gout + (size_t)(64 * panel + 16 * w + tok) * D + 80 * slice + 16 * t + 4 * q) = o;
    } else {
      *(v2u*)(ws + X2F_OFF + xoff[t]) = o;
    }
  }
  STAMP(6);
  STAMP_OUT(LAST ? 4 : 2);
}

}

extern "C" void kernel_launch(void* const* d_in, const int* in_sizes, int n_in, void* d_out, int out_size, void* d_ws,
                              size_t ws_size, hipStream_t stream) {
  const u16* q = (const u16*)d_in[0];
  const u16* k = (const u16*)d_in[1];
  const u16* v = (const u16*)d_in[2];
  const u16* shift_k = (const u16*)d_in[3];
  const u16* ssm_k = (const u16*)d_in[4];
  const u16* ssm_D = (const u16*)d_in[5];
  const u16* w1 = (const u16*)d_in[7];
  const u16* w2 = (const u16*)d_in[8];
  unsigned char* ws = (unsigned char*)d_ws;
  u16* out = (u16*)d_out;
  (void)hipFuncSetAttribute((const void*)k_conv_prep, hipFuncAttributeMaxDynamicSharedMemorySize, K1_LDS);
  (void)hipFuncSetAttribute((const void*)k_g1<true>, hipFuncAttributeMaxDynamicSharedMemorySize, G1_LDS);
  (void)hipFuncSetAttribute((const void*)k_g1<false>, hipFuncAttributeMaxDynamicSharedMemorySize, G1_LDS);
  (void)hipFuncSetAttribute((const void*)k_g2<true>, hipFuncAttributeMaxDynamicSharedMemorySize, G2_LDS);
  (void)hipFuncSetAttribute((const void*)k_g2<false>, hipFuncAttributeMaxDynamicSharedMemorySize, G2_LDS);
  k_kv<<<320, 256, 0, stream>>>(k, v, shift_k, ws);
  k_conv_prep<<<CONV_WGS + PREP_WGS, 256, K1_LDS, stream>>>(ssm_k, ssm_D, w1, w2, ws);
  k_g1<true><<<256, 512, G1_LDS, stream>>>(q, ws);
  k_g2<false><<<256, 256, G2_LDS, stream>>>(ws, out);
  k_g1<false><<<256, 512, G1_LDS, stream>>>(q, ws);
  k_g2<true><<<256, 256, G2_LDS, stream>>>(ws, out);
}
```

```cpp
#include <hip/hip_runtime.h>
#include <hip/hip_bf16.h>
#include <stdint.h>

#define GAS __attribute__((address_space(1)))
#define LAS __attribute__((address_space(3)))
typedef unsigned v4u __attribute__((ext_vector_type(4)));
typedef unsigned v2u __attribute__((ext_vector_type(2)));
typedef float f32x4 __attribute__((ext_vector_type(4)));
typedef float f32x2 __attribute__((ext_vector_type(2)));
typedef short bf16x8 __attribute__((ext_vector_type(8)));
typedef short s16x4 __attribute__((ext_vector_type(4)));
typedef __bf16 bf16x2_t __attribute__((ext_vector_type(2)));
typedef unsigned short u16;

namespace {

constexpr int S = 2048, D = 640, F = 1728;
constexpr int KS1 = D / 32;
constexpr int KS2 = F / 32;
constexpr size_t WF_BYTES = (size_t)D * F * 2;
constexpr size_t XF_BYTES = (size_t)S * D * 2;
constexpr size_t HF_BYTES = (size_t)S * F * 2;
constexpr size_t W1F_OFF = 0;
constexpr size_t W2F_OFF = W1F_OFF + WF_BYTES;
constexpr size_t YF_OFF = W2F_OFF + WF_BYTES;
constexpr size_t X1F_OFF = YF_OFF + XF_BYTES;
constexpr size_t X2F_OFF = X1F_OFF + XF_BYTES;
constexpr size_t HF_OFF = X2F_OFF + XF_BYTES;
constexpr size_t KVT_OFF = HF_OFF + HF_BYTES;
constexpr size_t CS_OFF = KVT_OFF + XF_BYTES;
constexpr size_t H2F_OFF = CS_OFF + 32768;
constexpr size_t HC_OFF = H2F_OFF + HF_BYTES;
constexpr size_t CTL_OFF = HC_OFF + 4096;
constexpr size_t ZERO_BYTES = 3 * 32 * 256;
constexpr size_t WS_BYTES = CTL_OFF + ZERO_BYTES;

#define MAP84 1
#define PANEL_OF(b) ((((b) & 7) >> 1) * 8 + ((b) >> 5))
#define SLICE_OF(b) (((b) & 1) * 4 + (((b) >> 3) & 3))
__device__ __forceinline__ unsigned pack2(float a, float b) {
  f32x2 v = {a, b};
  bf16x2_t r = __builtin_convertvector(v, bf16x2_t);
  return __builtin_bit_cast(unsigned, r);
}
__device__ __forceinline__ float lo16(unsigned u) { return __builtin_bit_cast(float, u << 16); }
__device__ __forceinline__ float hi16(unsigned u) { return __builtin_bit_cast(float, u & 0xffff0000u); }
__device__ __forceinline__ float rbf(float a) { return lo16(pack2(a, a)); }
__device__ __forceinline__ f32x4 mfma(v4u a, v4u b, f32x4 c) {
  return __builtin_amdgcn_mfma_f32_16x16x32_bf16(__builtin_bit_cast(bf16x8, a), __builtin_bit_cast(bf16x8, b), c, 0, 0, 0);
}
__device__ __forceinline__ v2u tr_read(LAS unsigned char* p) {
  return __builtin_bit_cast(v2u, __builtin_amdgcn_ds_read_tr16_b64_v4i16((LAS s16x4*)p));
}

#define WAVE_SYNC()                                      \
  do {                                                   \
    __builtin_amdgcn_fence(__ATOMIC_RELEASE, "wavefront"); \
    __builtin_amdgcn_wave_barrier();                     \
    __builtin_amdgcn_fence(__ATOMIC_ACQUIRE, "wavefront"); \
  } while (0)
constexpr int K0_RSTR = 144;
constexpr int K0_KV = 320, K0_H = 160;
constexpr int K0_GRID = K0_KV + K0_H;
__global__ __launch_bounds__(256) void k_prep(const u16* __restrict__ gk, const u16* __restrict__ gv,
                                              const u16* __restrict__ gshift, const u16* __restrict__ gssm,
                                              unsigned char* __restrict__ ws) {
  __shared__ __attribute__((aligned(16))) unsigned char tile_raw[64 * K0_RSTR];
  LAS unsigned char* tile = (LAS unsigned char*)tile_raw;
  const int b = blockIdx.x, tid = threadIdx.x;
  if (b < K0_KV) {
    if ((size_t)(b * 256 + tid) * 16 < ZERO_BYTES) *(v4u*)(ws + CTL_OFF + (size_t)(b * 256 + tid) * 16) = v4u{0u, 0u, 0u, 0u};
    const int t0 = (b & 31) * 64, d0 = (b >> 5) * 64;
    const int tr = tid >> 3, c8 = tid & 7;
    const int tb = t0 + 2 * tr, dc = d0 + 8 * c8;
    v4u kr[5], vr[2], sw[4];
#pragma unroll
    for (int r = 0; r < 5; ++r) {
      const int t = tb - 3 + r;
      kr[r] = *(const v4u*)(gk + (size_t)(t < 0 ? 0 : t) * D + dc);
    }
#pragma unroll
    for (int r = 0; r < 2; ++r) vr[r] = *(const v4u*)(gv + (size_t)(tb + r) * D + dc);
#pragma unroll
    for (int r = 0; r < 4; ++r) sw[r] = *(const v4u*)(gshift + dc * 4 + 8 * r);
#pragma unroll
    for (int r = 0; r < 3; ++r)
      if (tb - 3 + r < 0) kr[r] = v4u{0u, 0u, 0u, 0u};
#pragma unroll
    for (int j = 0; j < 8; ++j) {
      const unsigned wa = sw[j >> 1][2 * (j & 1)], wb = sw[j >> 1][2 * (j & 1) + 1];
      const float w0 = lo16(wa), w1 = hi16(wa), w2 = lo16(wb), w3 = hi16(wb);
      float kf[5];
#pragma unroll
      for (int r = 0; r < 5; ++r) kf[r] = (j & 1) ? hi16(kr[r][j >> 1]) : lo16(kr[r][j >> 1]);
      const float v0 = (j & 1) ? hi16(vr[0][j >> 1]) : lo16(vr[0][j >> 1]);
      const float v1 = (j & 1) ? hi16(vr[1][j >> 1]) : lo16(vr[1][j >> 1]);
      const float ks0 = fmaf(kf[3], w0, fmaf(kf[2], w1, fmaf(kf[1], w2, kf[0] * w3)));
      const float ks1 = fmaf(kf[4], w0, fmaf(kf[3], w1, fmaf(kf[2], w2, kf[1] * w3)));
      const unsigned kp = pack2(ks0, ks1);
      *(LAS unsigned*)(tile + (8 * c8 + j) * K0_RSTR + 124 - 4 * tr) = pack2(hi16(kp) * v1, lo16(kp) * v0);
    }
    __syncthreads();
    const int ch = tid >> 2, part = tid & 3;
    const v4u o0 = *(const LAS v4u*)(tile + ch * K0_RSTR + part * 32);
    const v4u o1 = *(const LAS v4u*)(tile + ch * K0_RSTR + part * 32 + 16);
    v4u* dst = (v4u*)(ws + KVT_OFF + ((size_t)(d0 + ch) * S + (1984 - t0) + 16 * part) * 2);
    dst[0] = o0;
    dst[1] = o1;
    return;
  }
  {
    const int bb = b - K0_KV;
    const int cg = (bb & 7) * 20 + (bb >> 3);
    const v4u* src = (const v4u*)(gssm + (size_t)(4 * cg) * S) + tid;
    v4u x = src[0] ^ src[256] ^ src[512] ^ src[768];
    if ((x.x ^ x.y ^ x.z ^ x.w) == 0x9e3779b9u) *(unsigned*)(ws + HC_OFF) = 0u;
  }
}

#define PREP_ST(v, p) __builtin_amdgcn_raw_buffer_store_b128((v), prs, (unsigned)((unsigned char*)(p) - ws), 0, 16)
constexpr int P_W1 = 27, P_W2 = 34, P_Q = 32;
__device__ __forceinline__ void prep_weights(int b, int tid, int lane, int w, LAS unsigned char* tile, const u16* __restrict__ gw1,
                                             const u16* __restrict__ gw2, const u16* __restrict__ gq,
                                             unsigned char* __restrict__ ws) {
  const __amdgpu_buffer_rsrc_t prs = __builtin_amdgcn_make_buffer_rsrc(ws, 0, (int)WS_BYTES, 0x00020000);
  LAS unsigned char* tl = tile + w * (4 * 32 * 144);
  const int r8 = lane >> 3, c8 = lane & 7;
  const int g = lane >> 4, qq = (lane & 15) >> 2, pp = lane & 3;
  if (b < P_W1) {
    const int nb = b, kq = w;
    v4u ld[5][4];
#pragma unroll
    for (int r = 0; r < 5; ++r) {
      const u16* src = gw1 + (size_t)(5 * kq + r) * 32 * F + nb * 64;
#pragma unroll
      for (int u = 0; u < 4; ++u) ld[r][u] = *(const v4u*)(src + (size_t)(8 * u + r8) * F + c8 * 8);
    }
    f32x4 cs[4];
#pragma unroll
    for (int t = 0; t < 4; ++t) cs[t] = f32x4{0.f, 0.f, 0.f, 0.f};
    const v4u ones = v4u{0x3f803f80u, 0x3f803f80u, 0x3f803f80u, 0x3f803f80u};
#pragma unroll
    for (int r = 0; r < 5; ++r) {
      LAS unsigned char* tr = tl + (r & 1) * (32 * 144);
      WAVE_SYNC();
#pragma unroll
      for (int u = 0; u < 4; ++u) *(LAS v4u*)(tr + (8 * u + r8) * 144 + c8 * 16) = ld[r][u];
      WAVE_SYNC();
      const int i = 5 * kq + r;
#pragma unroll
      for (int t = 0; t < 4; ++t) {
        const v2u lo = tr_read(tr + (8 * g + qq) * 144 + (16 * t + 4 * pp) * 2);
        const v2u hi = tr_read(tr + (8 * g + 4 + qq) * 144 + (16 * t + 4 * pp) * 2);
        const int T = 4 * nb + t;
        const v4u f = v4u{lo.x, lo.y, hi.x, hi.y};
        PREP_ST(f, (v4u*)(ws + W1F_OFF + (size_t)(((T >> 1) * KS1 + i) * 2 + (T & 1)) * 1024 + lane * 16));
        cs[t] = mfma(f, ones, cs[t]);
      }
    }
    __syncthreads();
    LAS f32x4* cr = (LAS f32x4*)tile;
    if ((lane & 15) == 0) {
#pragma unroll
      for (int t = 0; t < 4; ++t) cr[kq * 16 + 4 * t + g] = cs[t];
    }
    __syncthreads();
    if (tid < 16) *(f32x4*)(ws + CS_OFF + ((size_t)nb * 64 + 4 * tid) * 4) = (cr[tid] + cr[16 + tid]) + (cr[32 + tid] + cr[48 + tid]);
  } else if (b < P_W1 + P_W2) {
    const int gw = (b - P_W1) * 4 + w;
    if (gw < 135) {
      v4u ld[4][4];
#pragma unroll
      for (int r = 0; r < 4; ++r) {
        const int id = gw * 4 + r;
        const u16* src = gw2 + (size_t)(id / 10) * 32 * D + (id % 10) * 64;
#pragma unroll
        for (int u = 0; u < 4; ++u) ld[r][u] = *(const v4u*)(src + (size_t)(8 * u + r8) * D + c8 * 8);
      }
#pragma unroll
      for (int r = 0; r < 4; ++r) {
        const int id = gw * 4 + r;
        LAS unsigned char* tr = tl + r * (32 * 144);
#pragma unroll
        for (int u = 0; u < 4; ++u) *(LAS v4u*)(tr + (8 * u + r8) * 144 + c8 * 16) = ld[r][u];
        WAVE_SYNC();
        const int i = id / 10, db = id % 10;
#pragma unroll
        for (int t = 0; t < 4; ++t) {
          const v2u lo = tr_read(tr + (4 * g + qq) * 144 + (16 * t + 4 * pp) * 2);
          const v2u hi = tr_read(tr + (16 + 4 * g + qq) * 144 + (16 * t + 4 * pp) * 2);
          const int T = 4 * db + t;
          PREP_ST((v4u{lo.x, lo.y, hi.x, hi.y}), (v4u*)(ws + W2F_OFF + (size_t)(((T / 5) * KS2 + i) * 5 + (T % 5)) * 1024 + lane * 16));
        }
      }
    }
  } else if (b < P_W1 + P_W2 + P_Q) {
    const int tt = (b - P_W1 - P_W2) * 4 + w;
    const u16* xp = gq + (size_t)(16 * tt + (lane & 15)) * D + 8 * g;
    v4u x[KS1];
#pragma unroll
    for (int i = 0; i < KS1; ++i) x[i] = *(const v4u*)(xp + 32 * i);
    v4u* xo = (v4u*)(ws + X1F_OFF) + ((size_t)tt * KS1) * 64 + lane;
#pragma unroll
    for (int i = 0; i < KS1; ++i) PREP_ST(x[i], xo + i * 64);
  }
}

constexpr int CONV_WGS = 160;
constexpr int XR_BYTES = 4608;
constexpr int RS = 4160;
constexpr int CH_BYTES = XR_BYTES + 4 * RS;
constexpr int K1_LDS = 4 * CH_BYTES;

__global__ __launch_bounds__(256, 1) void k_conv(const u16* __restrict__ gssm, const u16* __restrict__ gD,
                                                 const u16* __restrict__ gw1, const u16* __restrict__ gw2,
                                                 const u16* __restrict__ gq, unsigned char* __restrict__ ws) {
  extern __shared__ __attribute__((aligned(16))) unsigned char smem_raw[];
  LAS unsigned char* smem = (LAS unsigned char*)smem_raw;
  const int b = blockIdx.x, tid = threadIdx.x, lane = tid & 63;
  const int w = __builtin_amdgcn_readfirstlane(tid >> 6);
  if (b >= CONV_WGS) {
    prep_weights(b - CONV_WGS, tid, lane, w, smem, gw1, gw2, gq, ws);
  } else {
    const int cg = (b & 7) * 20 + (b >> 3), d0 = 4 * cg;
    const u16* hrow = gssm + (size_t)(d0 + w) * S;
    const v4u* xrow = (const v4u*)(ws + KVT_OFF + (size_t)(d0 + w) * S * 2);
    v4u ha[4], xa[4];
    v2u hn[4];
#pragma unroll
    for (int u = 0; u < 4; ++u) {
      const int c = 64 * u + lane;
      ha[u] = *(const v4u*)(hrow + 8 * c);
      hn[u] = *(const v2u*)(hrow + 8 * (c < 255 ? c + 1 : c));
    }
#pragma unroll
    for (int u = 0; u < 4; ++u) xa[u] = xrow[64 * u + lane];
    const unsigned Dbits = gD[d0 + w];
    __builtin_amdgcn_sched_barrier(0);
    if (lane == 63) hn[3] = v2u{0u, 0u};

    LAS unsigned char* chb = smem + w * CH_BYTES;
    LAS unsigned char* hs = chb + XR_BYTES;
#pragma unroll
    for (int u = 0; u < 4; ++u) {
      const int c = 64 * u + lane;
      const v4u a = ha[u];
      const v2u n = hn[u];
      const unsigned s01 = __builtin_amdgcn_alignbyte(a.y, a.x, 2), s12 = __builtin_amdgcn_alignbyte(a.z, a.y, 2),
                     s23 = __builtin_amdgcn_alignbyte(a.w, a.z, 2), s34 = __builtin_amdgcn_alignbyte(n.x, a.w, 2),
                     s45 = __builtin_amdgcn_alignbyte(n.y, n.x, 2);
      LAS unsigned char* dst = hs + (c + 2) * 16;
      *(LAS v4u*)(dst) = a;
      *(LAS v4u*)(dst + RS) = v4u{s01, s12, s23, s34};
      *(LAS v4u*)(dst + 2 * RS) = v4u{a.y, a.z, a.w, n.x};
      *(LAS v4u*)(dst + 3 * RS) = v4u{s12, s23, s34, s45};
    }
    {
      const unsigned f0 = __builtin_amdgcn_readfirstlane(ha[0].x), f1 = __builtin_amdgcn_readfirstlane(ha[0].y);
      if (lane < 2) {
        const unsigned n0 = lane ? f0 : 0u, n1 = lane ? f1 : 0u;
        LAS unsigned char* dst = hs + lane * 16;
        *(LAS v4u*)(dst) = v4u{0u, 0u, 0u, 0u};
        *(LAS v4u*)(dst + RS) = v4u{0u, 0u, 0u, n0 << 16};
        *(LAS v4u*)(dst + 2 * RS) = v4u{0u, 0u, 0u, n0};
        *(LAS v4u*)(dst + 3 * RS) = v4u{0u, 0u, n0 << 16, __builtin_amdgcn_alignbyte(n1, n0, 2)};
      }
      if (lane < 30) *(LAS v4u*)(chb + 4096 + lane * 16) = v4u{0u, 0u, 0u, 0u};
    }
#pragma unroll
    for (int u = 0; u < 4; ++u) *(LAS v4u*)(chb + (64 * u + lane) * 16) = xa[u];
    WAVE_SYNC();
    const int i16 = lane & 15, q = lane >> 4;
    const int sig = (1 + i16) & 3;
    LAS unsigned char* abase = hs + sig * RS + 2 * (1 + i16 - sig) + 16 * q;
    LAS unsigned char* bbase = chb + 480 - 32 * i16 + 16 * q;
    v4u B[2][8];
#pragma unroll
    for (int ww = 0; ww < 8; ++ww) B[0][ww] = *(LAS v4u*)(bbase + 64 * (56 + ww));
    v4u A[64];
#pragma unroll
    for (int c = 0; c < 64; ++c) {
      const v2u lo = *(volatile LAS v2u*)(abase + 64 * c);
      const v2u hi = *(volatile LAS v2u*)(abase + 64 * c + 8);
      A[c] = v4u{lo.x, lo.y, hi.x, hi.y};
    }
    v4u Ad;
    {
      const int j = 15 - i16 - 8 * q;
      const unsigned dv = (j & 1) ? (Dbits << 16) : Dbits;
#pragma unroll
      for (int c = 0; c < 4; ++c) Ad[c] = (j >= 0 && (j >> 1) == c) ? dv : 0u;
    }
    f32x4 acc[8];
#pragma unroll
    for (int n = 0; n < 8; ++n) acc[n] = f32x4{0.f, 0.f, 0.f, 0.f};
#pragma unroll
    for (int v = 0; v < 8; ++v) {
      if (v < 7) {
#pragma unroll
        for (int ww = 0; ww < 8; ++ww) B[(v + 1) & 1][ww] = *(LAS v4u*)(bbase + 64 * (56 - (8 * (v + 1) - ww)));
      }
      __builtin_amdgcn_sched_barrier(0);
      acc[v] = mfma(Ad, B[v & 1][0], acc[v]);
#pragma unroll
      for (int u = 0; u < 8; ++u) {
        if (u + v < 8) {
#pragma unroll
          for (int ww = 0; ww < 8; ++ww) acc[u + v] = mfma(A[8 * u + ww], B[v & 1][ww], acc[u + v]);
        }
      }
      __builtin_amdgcn_sched_barrier(0);
    }
    LAS unsigned char* yt = hs + 64 * w;
#pragma unroll
    for (int n = 0; n < 8; ++n) {
      const int s0 = 256 * n + 16 * i16 + 4 * q;
      *(LAS v2u*)(yt + s0 * 2) = v2u{pack2(acc[n][0], acc[n][1]), pack2(acc[n][2], acc[n][3])};
    }
    __syncthreads();
    {
      const int qq = (lane & 15) >> 2, pp = lane & 3;
      LAS unsigned char* tb = smem + qq * CH_BYTES + XR_BYTES + 64 * qq + (16 * q + 4 * pp) * 2;
      unsigned char* yf = ws + YF_OFF + (size_t)(d0 >> 5) * 1024 + ((d0 >> 3) & 3) * 256 + ((d0 >> 2) & 1) * 8 + (lane & 15) * 16;
#pragma unroll
      for (int it = 0; it < 8; ++it) {
        const int tok0 = (w * 8 + it) * 64;
        const v2u r = tr_read(tb + tok0 * 2);
        const int tt = (tok0 >> 4) + q;
        *(v2u*)(yf + (size_t)tt * (KS1 * 1024)) = r;
      }
    }
  }
}

#define G1_NG 20
#define G1_NG2 12
#define G1_NL 2
constexpr int G1_A_BYTES = 4 * KS1 * 1024;
constexpr int G1_ST_OFF = G1_A_BYTES;

__device__ __forceinline__ f32x2 finish_stats(float sum, float ssq) {
  const float mean = sum * (1.f / 640.f);
  const float var = fmaxf(ssq * (1.f / 640.f) - mean * mean, 0.f);
  const float mb = rbf(mean);
  const float sd = rbf(__builtin_amdgcn_sqrtf(rbf(rbf(var) + 1e-5f)));
  const float rs = __builtin_amdgcn_rcpf(sd);
  return f32x2{rs, -mb * rs};
}

#define LDS_BARRIER()                                               \
  do {                                                              \
    __builtin_amdgcn_fence(__ATOMIC_RELEASE, "workgroup", "local"); \
    __builtin_amdgcn_s_barrier();                                   \
    __builtin_amdgcn_fence(__ATOMIC_ACQUIRE, "workgroup", "local"); \
  } while (0)

__device__ __forceinline__ v4u g1_silu(f32x4 a0, f32x4 a1, f32x2 st, f32x4 c0, f32x4 c1) {
  v4u o;
  const f32x4 rs = {st.x, st.x, st.x, st.x};
#pragma unroll
  for (int s = 0; s < 2; ++s) {
    const f32x4 z = __builtin_elementwise_fma(s ? a1 : a0, rs, (s ? c1 : c0) * st.y);
    const f32x4 zl = z * (-1.44269504f);
    f32x4 e;
#pragma unroll
    for (int r = 0; r < 4; ++r) e[r] = __builtin_amdgcn_exp2f(zl[r]);
    e = e + 1.f;
#pragma unroll
    for (int r = 0; r < 4; ++r) e[r] = __builtin_amdgcn_rcpf(e[r]);
    const f32x4 h = z * e;
    o[2 * s] = pack2(h[0], h[1]);
    o[2 * s + 1] = pack2(h[2], h[3]);
  }
  return o;
}

#define YLOAD(p) __builtin_amdgcn_raw_buffer_load_b128(rs, (unsigned)((const unsigned char*)(p) - ws), 0, 0)
typedef GAS unsigned gu32;
#define RLX_AGENT __ATOMIC_RELAXED, __HIP_MEMORY_SCOPE_AGENT
constexpr int AUX_SC1 = 16;
constexpr unsigned SPIN_CAP = 1u << 18;
constexpr int FFN_RED_BYTES = 20 * 7 * 1024;
constexpr int FFN_LDS = FFN_RED_BYTES;
typedef __amdgpu_buffer_rsrc_t rsrc_t;

__device__ __forceinline__ v4u ld_sc1(rsrc_t rs, unsigned off) { return __builtin_amdgcn_raw_buffer_load_b128(rs, off, 0, AUX_SC1); }
__device__ __forceinline__ void st_sc1(rsrc_t rs, unsigned off, v4u v) { __builtin_amdgcn_raw_buffer_store_b128(v, rs, off, 0, AUX_SC1); }
__device__ __forceinline__ void st_sc1_64(rsrc_t rs, unsigned off, v2u v) { __builtin_amdgcn_raw_buffer_store_b64(v, rs, off, 0, AUX_SC1); }

__device__ __forceinline__ void publish(gu32* cnt) {
  asm volatile("s_waitcnt vmcnt(0)" ::: "memory");
  LDS_BARRIER();
  if (threadIdx.x == 0) __hip_atomic_fetch_add(cnt, 1u, RLX_AGENT);
}
__device__ __forceinline__ void await8(gu32* cnt) {
  if (threadIdx.x == 0) {
    for (unsigned spins = 0; spins < SPIN_CAP; ++spins) {
      if (__hip_atomic_load(cnt, RLX_AGENT) >= 8u) break;
      __builtin_amdgcn_s_sleep(1);
    }
    __builtin_amdgcn_fence(__ATOMIC_ACQUIRE, "wavefront");
  }
  LDS_BARRIER();
  __builtin_amdgcn_fence(__ATOMIC_ACQUIRE, "wavefront");
}

#define G2_WPF 4
#define G2_HPF 3
template <bool FIRST, bool ACTIVE>
__device__ __forceinline__ void g1_phase(const u16* __restrict__ gq, unsigned char* __restrict__ ws, rsrc_t rs,
                                         LAS unsigned char* smem, gu32* cnt_in, int panel, int slice, int P, int w, int lane,
                                         v2u (&xres)[3], v4u (&pre)[32]) {
  asm volatile("" : "+v"(lane));
  const int tt = w & 3, kh = w >> 2;
  const int tok = lane & 15, g = lane >> 4;
  const v4u* wp = (const v4u*)(ws + W1F_OFF) + (size_t)P * (KS1 * 128) + lane;
  v4u wf[40];
#define G1_WLOAD(f) do { if (ACTIVE && (f) < 40) wf[f] = wp[(f) * 64]; } while (0)
  const v4u ones = v4u{0x3f803f80u, 0x3f803f80u, 0x3f803f80u, 0x3f803f80u};
  LAS f32x2* st = (LAS f32x2*)(smem + G1_ST_OFF);
  LAS v4u* awp = (LAS v4u*)smem + (tt * KS1 + 10 * kh) * 64 + lane;
  v4u xr[10];
  f32x4 sx = {0.f, 0.f, 0.f, 0.f}, gx = sx;
  constexpr int NPRE = FIRST ? 0 : G1_NG2;
  constexpr int NGATE = FIRST ? G1_NG : G1_NG2 + 6;
  v4u yr[10];
  const v4u* yp = (const v4u*)(ws + YF_OFF) + ((size_t)(4 * panel + tt) * KS1 + 10 * kh) * 64 + lane;
  if (FIRST) {
    const v4u* xp = (const v4u*)(ws + X1F_OFF) + ((size_t)(4 * panel + tt) * KS1 + 10 * kh) * 64 + lane;
#pragma unroll
    for (int i = 0; i < 10; ++i) xr[i] = xp[i * 64];
    __builtin_amdgcn_sched_barrier(0);
    __builtin_amdgcn_s_barrier();
    __builtin_amdgcn_sched_barrier(0);
#pragma unroll
    for (int i = 0; i < 10; ++i) yr[i] = YLOAD(yp + i * 64);
    __builtin_amdgcn_sched_barrier(0);
  } else {
#pragma unroll
    for (int i = 0; i < 10; ++i) yr[i] = pre[22 + i];
    if (ACTIVE) {
#pragma unroll
      for (int f = 0; f < NPRE; ++f) wf[f] = pre[f];
    }
    await8(cnt_in);
    const unsigned xo = (unsigned)(X2F_OFF + (((size_t)(4 * panel + tt) * KS1 + 10 * kh) * 64 + lane) * 16);
#pragma unroll
    for (int i = 0; i < 10; ++i) xr[i] = ld_sc1(rs, xo + i * 1024);
    __builtin_amdgcn_sched_barrier(0);
  }
  {
    f32x4 sa = {0.f, 0.f, 0.f, 0.f}, ga = sa, sb = sa, gb = sa;
#pragma unroll
    for (int i = 0; i < 10; i += 2) {
      sa = mfma(ones, xr[i], sa);
      ga = mfma(xr[i], xr[i], ga);
      sb = mfma(ones, xr[i + 1], sb);
      gb = mfma(xr[i + 1], xr[i + 1], gb);
    }
    ga += gb;
    const int r = tok & 3;
    const float ss = r == 0 ? ga[0] : (r == 1 ? ga[1] : (r == 2 ? ga[2] : ga[3]));
    if (g == (tok >> 2)) st[((0 * 2 + kh) * 4 + tt) * 16 + tok] = f32x2{sa[0] + sb[0], ss};
  }
  __builtin_amdgcn_sched_barrier(0);
  LDS_BARRIER();
  {
    const f32x2 pa = st[((0 * 2 + 0) * 4 + tt) * 16 + tok], pb = st[((0 * 2 + 1) * 4 + tt) * 16 + tok];
    const f32x2 s1 = finish_stats(pa.x + pb.x, pa.y + pb.y);
    const f32x2 rsv = {s1.x, s1.x}, nm = {s1.y, s1.y};
#pragma unroll
    for (int i = 0; i < 10; ++i) {
#pragma unroll
      for (int f = NPRE + ((NGATE - NPRE) * i) / 10; f < NPRE + ((NGATE - NPRE) * (i + 1)) / 10; ++f) G1_WLOAD(f);
#pragma unroll
      for (int c = 0; c < 4; ++c) {
        const f32x2 xv = {lo16(xr[i][c]), hi16(xr[i][c])};
        const f32x2 yv = {lo16(yr[i][c]), hi16(yr[i][c])};
        const f32x2 t = __builtin_elementwise_fma(xv, rsv, nm);
        const f32x2 o = __builtin_elementwise_fma(t, yv, xv);
        xr[i][c] = pack2(o.x, o.y);
      }
      awp[i * 64] = xr[i];
      sx = mfma(ones, xr[i], sx);
      gx = mfma(xr[i], xr[i], gx);
      __builtin_amdgcn_sched_barrier(0);
    }
  }
  {
    const int r = tok & 3;
    const float ss = r == 0 ? gx[0] : (r == 1 ? gx[1] : (r == 2 ? gx[2] : gx[3]));
    if (g == (tok >> 2)) st[((1 * 2 + kh) * 4 + tt) * 16 + tok] = f32x2{sx[0], ss};
  }
  LDS_BARRIER();
#pragma unroll
  for (int j = 0; j < 3; ++j) {
    const int T = 5 * slice + 3 * kh + (j < 2 ? j : (kh ? 1 : 2));
    xres[j] = *(LAS v2u*)(smem + ((tt * KS1 + (T >> 1)) * 64 + tok + 16 * (2 * (T & 1) + (g >> 1))) * 16 + 8 * (g & 1));
  }
  const v4u* w2p = (const v4u*)(ws + W2F_OFF) + ((size_t)(slice * KS2 + w) * 5) * 64 + lane;
#define G1_W2LOAD(f) do { if ((f) < 5 * G2_WPF) pre[f] = w2p[(((f) / 5) * 40 + (f) % 5) * 64]; } while (0)
  if (!ACTIVE) {
#pragma unroll
    for (int f = 0; f < 5 * G2_WPF; ++f) G1_W2LOAD(f);
    return;
  }
  f32x4 acc[2][4];
#pragma unroll
  for (int s = 0; s < 2; ++s)
#pragma unroll
    for (int t = 0; t < 4; ++t) acc[s][t] = f32x4{0.f, 0.f, 0.f, 0.f};
  const LAS v4u* ap = (const LAS v4u*)smem + lane;
  v4u a[2][4];
#pragma unroll
  for (int t = 0; t < 4; ++t) a[0][t] = ap[(t * KS1) * 64];
  const f32x4 c0 = ((const f32x4*)(ws + CS_OFF))[8 * P + g], c1 = ((const f32x4*)(ws + CS_OFF))[8 * P + 4 + g];
  __builtin_amdgcn_sched_barrier(0);
#pragma unroll
  for (int i = 0; i < KS1; ++i) {
    if (i + 1 < KS1) {
#pragma unroll
      for (int t = 0; t < 4; ++t) a[(i + 1) & 1][t] = ap[(t * KS1 + i + 1) * 64];
    }
#pragma unroll
    for (int f = NGATE + G1_NL * i; f < NGATE + G1_NL * (i + 1); ++f) G1_WLOAD(f);
    if (i >= 10) {
      G1_W2LOAD(2 * (i - 10));
      G1_W2LOAD(2 * (i - 10) + 1);
    }
#pragma unroll
    for (int t = 0; t < 4; ++t) {
      acc[0][t] = mfma(wf[2 * i], a[i & 1][t], acc[0][t]);
      acc[1][t] = mfma(wf[2 * i + 1], a[i & 1][t], acc[1][t]);
    }
#pragma unroll
    for (int r = 0; r < 4; ++r) {
      __builtin_amdgcn_sched_group_barrier(0x8, 1, 0);
      __builtin_amdgcn_sched_group_barrier(0x100, 1, 0);
      __builtin_amdgcn_sched_group_barrier(0x8, 1, 0);
      __builtin_amdgcn_sched_group_barrier(0x20, 1, 0);
    }
    __builtin_amdgcn_sched_barrier(0);
  }
  f32x2 sp[8];
#pragma unroll
  for (int t = 0; t < 8; ++t) sp[t] = st[(2 * 4 + t) * 16 + tok];
  const unsigned ho = (unsigned)((FIRST ? HF_OFF : H2F_OFF) + (((size_t)(panel * KS2 + P) * 4) * 64 + lane) * 16);
#pragma unroll
  for (int t = 0; t < 4; ++t) {
    const f32x2 s2 = finish_stats(sp[t].x + sp[4 + t].x, sp[t].y + sp[4 + t].y);
    st_sc1(rs, ho + t * 1024, g1_silu(acc[0][t], acc[1][t], s2, c0, c1));
  }
}

template <bool LAST>
__device__ __forceinline__ void g2_phase(unsigned char* __restrict__ ws, rsrc_t rs, LAS unsigned char* smem, gu32* cnt_in,
                                         u16* __restrict__ gout, int panel, int slice, int w, int lane, int P, v2u (&xres)[3],
                                         v4u (&pre)[32]) {
  asm volatile("" : "+v"(lane));
  const int tok = lane & 15, q = lane >> 4;
  const int ta = w & 3, hi = w >> 2;
  const int j6 = w < 6 ? 6 : 5;
  const v4u* wp = (const v4u*)(ws + W2F_OFF) + ((size_t)(slice * KS2 + w) * 5) * 64 + lane;
  const v4u* wp6 = wp + (size_t)(j6 - 6) * 40 * 64;
  const unsigned ho = (unsigned)((LAST ? H2F_OFF : HF_OFF) + (((size_t)(panel * KS2 + w) * 4) * 64 + lane) * 16);
  const unsigned ho6 = ho + (unsigned)(j6 - 6) * 32 * 1024;
  v4u hf[7][4], wf[7][5];
#define G2_WLOAD(j) _Pragma("unroll") for (int t = 0; t < 5; ++t) wf[j][t] = ((j) == 6 ? wp6 : wp)[((j) * 40 + t) * 64];
#define G2_HLOAD(j) _Pragma("unroll") for (int t = 0; t < 4; ++t) hf[j][t] = ld_sc1(rs, ((j) == 6 ? ho6 : ho) + ((j) * 32 + t) * 1024);
#pragma unroll
  for (int j = 0; j < G2_WPF; ++j)
#pragma unroll
    for (int t = 0; t < 5; ++t) wf[j][t] = pre[5 * j + t];
  __builtin_amdgcn_sched_barrier(0);
  await8(cnt_in);
#pragma unroll
  for (int j = 0; j < G2_HPF; ++j) { G2_HLOAD(j) }
  __builtin_amdgcn_sched_barrier(0);
  f32x4 acc[4][5];
#pragma unroll
  for (int a = 0; a < 4; ++a)
#pragma unroll
    for (int t = 0; t < 5; ++t) acc[a][t] = f32x4{0.f, 0.f, 0.f, 0.f};
#pragma unroll
  for (int j = 0; j < 7; ++j) {
    if (j < 6 || w < 6) {
#pragma unroll
      for (int t = 0; t < 5; ++t)
#pragma unroll
        for (int a = 0; a < 4; ++a) acc[a][t] = mfma(wf[j][t], hf[j][a], acc[a][t]);
    }
    __builtin_amdgcn_sched_barrier(0);
    if (j + G2_HPF < 7) { G2_HLOAD(j + G2_HPF) }
    if (j + G2_WPF < 7) { G2_WLOAD(j + G2_WPF) }
    __builtin_amdgcn_sched_barrier(0);
  }
  if (!LAST) {
    const v4u* yp = (const v4u*)(ws + YF_OFF) + ((size_t)(4 * panel + ta) * KS1 + 10 * hi) * 64 + lane;
#pragma unroll
    for (int i = 0; i < 10; ++i) pre[22 + i] = YLOAD(yp + i * 64);
    const v4u* w1p = (const v4u*)(ws + W1F_OFF) + (size_t)P * (KS1 * 128) + lane;
#pragma unroll
    for (int f = 0; f < G1_NG2; ++f) pre[f] = w1p[f * 64];
    __builtin_amdgcn_sched_barrier(0);
  }
  LAS f32x4* red = (LAS f32x4*)smem;
#pragma unroll
  for (int a = 0; a < 4; ++a)
#pragma unroll
    for (int h2 = 0; h2 < 2; ++h2) {
      const int owner = a + 4 * h2;
      if (owner != w) {
        const int slot = (w - owner - 1) & 7;
#pragma unroll
        for (int t = 3 * h2; t < (h2 ? 5 : 3); ++t) red[((a * 5 + t) * 7 + slot) * 64 + lane] = acc[a][t];
      }
    }
  f32x4 fin[3];
#pragma unroll
  for (int a = 0; a < 4; ++a) {
    if (ta == a) {
      if (hi) {
        fin[0] = acc[a][3]; fin[1] = acc[a][4]; fin[2] = acc[a][4];
      } else {
        fin[0] = acc[a][0]; fin[1] = acc[a][1]; fin[2] = acc[a][2];
      }
    }
  }
  LDS_BARRIER();
#pragma unroll
  for (int j = 0; j < 3; ++j) {
    const int t = 3 * hi + (j < 2 ? j : (hi ? 1 : 2));
    const LAS f32x4* rp = red + ((ta * 5 + t) * 7) * 64 + lane;
    f32x4 p[7];
#pragma unroll
    for (int s = 0; s < 7; ++s) p[s] = rp[s * 64];
    fin[j] += ((p[0] + p[1]) + (p[2] + p[3])) + ((p[4] + p[5]) + p[6]);
  }
  v2u o[3];
#pragma unroll
  for (int j = 0; j < 3; ++j)
    o[j] = v2u{pack2(lo16(xres[j].x) + rbf(fin[j][0]), hi16(xres[j].x) + rbf(fin[j][1])),
               pack2(lo16(xres[j].y) + rbf(fin[j][2]), hi16(xres[j].y) + rbf(fin[j][3]))};
#pragma unroll
  for (int pr = 0; pr < 2; ++pr) {
    if (pr == 0 || !hi) {
      const v2u a = o[2 * pr], c = o[pr ? 2 : 1];
      const auto rx = __builtin_amdgcn_permlane16_swap(a.x, c.x, false, false);
      const auto ry = __builtin_amdgcn_permlane16_swap(a.y, c.y, false, false);
      const v4u v = v4u{rx[0], ry[0], rx[1], ry[1]};
      const int t = 3 * hi + 2 * pr + (pr ? 0 : (q & 1));
      if (pr == 0 || (q & 1) == 0) {
        if (LAST) {
          *(v4u*)(gout + (size_t)(64 * panel + 16 * ta + tok) * D + 80 * slice + 16 * t + 8 * (q >> 1)) = v;
        } else {
          const int T = 5 * slice + t;
          st_sc1(rs, (unsigned)X2F_OFF + (unsigned)((((4 * panel + ta) * KS1 + (T >> 1)) * 64 + tok + 16 * (2 * (T & 1) + (q >> 1))) * 16), v);
        }
      }
    }
  }
}

__global__ __launch_bounds__(512, 2) void k_ffn(const u16* __restrict__ gq, unsigned char* __restrict__ ws, u16* __restrict__ gout) {
  extern __shared__ __attribute__((aligned(16))) unsigned char smem_raw[];
  LAS unsigned char* smem = (LAS unsigned char*)smem_raw;
  const int b = blockIdx.x, tid = threadIdx.x, lane = tid & 63;
  const int w = __builtin_amdgcn_readfirstlane(tid >> 6);
  const int panel = PANEL_OF(b), slice = SLICE_OF(b);
  const int pstart = slice < 6 ? 7 * slice : 42 + 6 * (slice - 6);
  const int npairs = slice < 6 ? 7 : 6;
  const rsrc_t rs = __builtin_amdgcn_make_buffer_rsrc(ws, 0, (int)WS_BYTES, 0x00020000);
  gu32* cnt = (gu32*)(ws + CTL_OFF) + panel * 64;
  v2u xres[3];
  v4u pre[32];
  const int P = pstart + (w < npairs ? w : 0);
  if (w < npairs)
    g1_phase<true, true>(gq, ws, rs, smem, cnt, panel, slice, pstart + w, w, lane, xres, pre);
  else
    g1_phase<true, false>(gq, ws, rs, smem, cnt, panel, slice, pstart, w, lane, xres, pre);
  publish(cnt);
  g2_phase<false>(ws, rs, smem, cnt, gout, panel, slice, w, lane, P, xres, pre);
  publish(cnt + 32 * 64);
  if (w < npairs)
    g1_phase<false, true>(gq, ws, rs, smem, cnt + 32 * 64, panel, slice, pstart + w, w, lane, xres, pre);
  else
    g1_phase<false, false>(gq, ws, rs, smem, cnt + 32 * 64, panel, slice, pstart, w, lane, xres, pre);
  publish(cnt + 2 * 32 * 64);
  g2_phase<true>(ws, rs, smem, cnt + 2 * 32 * 64, gout, panel, slice, w, lane, P, xres, pre);
}

}

extern "C" void kernel_launch(void* const* d_in, const int* in_sizes, int n_in, void* d_out, int out_size, void* d_ws,
                              size_t ws_size, hipStream_t stream) {
  const u16* q = (const u16*)d_in[0];
  const u16* k = (const u16*)d_in[1];
  const u16* v = (const u16*)d_in[2];
  const u16* shift_k = (const u16*)d_in[3];
  const u16* ssm_k = (const u16*)d_in[4];
  const u16* ssm_D = (const u16*)d_in[5];
  const u16* w1 = (const u16*)d_in[7];
  const u16* w2 = (const u16*)d_in[8];
  unsigned char* ws = (unsigned char*)d_ws;
  u16* out = (u16*)d_out;
  (void)hipFuncSetAttribute((const void*)k_conv, hipFuncAttributeMaxDynamicSharedMemorySize, K1_LDS);
  (void)hipFuncSetAttribute((const void*)k_ffn, hipFuncAttributeMaxDynamicSharedMemorySize, FFN_LDS);
  k_prep<<<K0_GRID, 256, 0, stream>>>(k, v, shift_k, ssm_k, ws);
  k_conv<<<CONV_WGS + P_W1 + P_W2 + P_Q, 256, K1_LDS, stream>>>(ssm_k, ssm_D, w1, w2, q, ws);
  k_ffn<<<256, 512, FFN_LDS, stream>>>(q, ws, out);
}
```
